```python
import torch

def _softmax(x, axis=-1):
    return torch.softmax(x, dim=axis)
def _gelu(x):
    return torch.nn.functional.gelu(x, approximate="tanh")
def _silu(x):
    return torch.nn.functional.silu(x)
def _sigmoid(x):
    return torch.sigmoid(x)
def _layernorm(x, axis=-1, eps=1e-5):
    m = x.mean(dim=axis, keepdim=True)
    v = x.var(dim=axis, keepdim=True, unbiased=False)
    return (x - m) / torch.sqrt(v + eps)
def _scatter_add(tgt, idx, src, axis=0):
    return tgt.clone().index_add_(axis, idx, src)
def _segment_sum(src, seg, n):
    out = torch.zeros((n,) + src.shape[1:], dtype=src.dtype)
    return out.index_add_(0, seg, src)
def _pad(x, pw):
    flat = tuple(v for pair in reversed(pw) for v in pair)
    return torch.nn.functional.pad(x, flat)

kernel_name = 'sequential_preLN__compressed_kv_cla__glu_gated_geglu__decode'

def setup_inputs():

    _w = (lambda a: (lambda t: t.to(torch.bfloat16) if t.is_floating_point() else t)(torch.as_tensor(a)))
    def _A_setup():
        B, H, Sq, Sk, D, M = 4, 32, 1, 8192, 128, 512
        Dc = 4 * D
        return {
            "x": _w((lambda *s: torch.randn(*s, dtype=torch.bfloat16) * 0.05)(B, Sq, M)),
            "c_kv": _w((lambda *s: torch.randn(*s, dtype=torch.bfloat16) * 0.8)(B, Sk, Dc)),
            "w_q": _w((lambda *s: torch.randn(*s, dtype=torch.bfloat16) * 1.6)(M, H, D) * (M ** -0.5)),
            "w_uk": _w((lambda *s: torch.randn(*s, dtype=torch.bfloat16) * 1.6)(Dc, H, D) * (Dc ** -0.5)),
            "w_uv": _w((lambda *s: torch.randn(*s, dtype=torch.bfloat16) * 0.8)(Dc, H, D) * (Dc ** -0.5)),
            "w_o": _w((lambda *s: torch.randn(*s, dtype=torch.bfloat16) * 0.8)(H, D, M) * ((H * D) ** -0.5)),
        }
    _A_in = _A_setup()

    def _B_setup():
        B, S, Dm, Df = 4, 1, 512, 1376
        return {
            "x": _w((lambda *s: torch.randn(*s, dtype=torch.bfloat16) * 0.05)(B, S, Dm)),
            "w_gate": _w((lambda *s: torch.randn(*s, dtype=torch.bfloat16) * 0.4)(Dm, Df) * (Dm ** -0.5)),
            "w_up": _w((lambda *s: torch.randn(*s, dtype=torch.bfloat16) * 0.4)(Dm, Df) * (Dm ** -0.5)),
            "w_down": _w((lambda *s: torch.randn(*s, dtype=torch.bfloat16) * 0.4)(Df, Dm) * (Df ** -0.5)),
        }
    _B_in = _B_setup()

    return {
        "A_x": _A_in["x"],
        "A_c_kv": _A_in["c_kv"],
        "A_w_q": _A_in["w_q"],
        "A_w_uk": _A_in["w_uk"],
        "A_w_uv": _A_in["w_uv"],
        "A_w_o": _A_in["w_o"],
        "B_x": _B_in["x"],
        "B_w_gate": _B_in["w_gate"],
        "B_w_up": _B_in["w_up"],
        "B_w_down": _B_in["w_down"],
    }

def reference(A_x, A_c_kv, A_w_q, A_w_uk, A_w_uv, A_w_o, B_x, B_w_gate, B_w_up, B_w_down):
    def _A_ref(x, c_kv, w_q, w_uk, w_uv, w_o):
        B, Sq, M = x.shape[0], x.shape[1], x.shape[2]
        Sk = c_kv.shape[1]
        H, D = w_q.shape[1], w_q.shape[2]
        q = torch.einsum("bqm,mhd->bhqd", x, w_q)
        k = torch.einsum("bkc,chd->bhkd", c_kv, w_uk)
        v = torch.einsum("bkc,chd->bhkd", c_kv, w_uv)
        scores = torch.einsum("bhqd,bhkd->bhqk", q, k) * (D ** -0.5)
        off = Sk - Sq
        qi = torch.arange(Sq)[:, None]
        kj = torch.arange(Sk)[None, :]
        cmask = kj <= (qi + off)
        scores = torch.where(cmask[None, None], scores, -1e9)
        p = _softmax(scores, axis=-1)
        ctx = torch.einsum("bhqk,bhkd->bhqd", p, v)
        return torch.einsum("bhqd,hdm->bqm", ctx, w_o)

    def _B_ref(x, w_gate, w_up, w_down):
        g = torch.einsum("bsd,df->bsf", x, w_gate)
        u = torch.einsum("bsd,df->bsf", x, w_up)
        h = _gelu(g) * u
        return torch.einsum("bsf,fd->bsd", h, w_down)

    x = A_x
    h = x + _A_ref(_layernorm(x, axis=-1), A_c_kv, A_w_q, A_w_uk, A_w_uv, A_w_o)
    h2 = h + _B_ref(_layernorm(h, axis=-1), B_w_gate, B_w_up, B_w_down)
    return h2
```

```cpp
#include <hip/hip_runtime.h>
#include <hip/hip_bf16.h>
#include <stdint.h>

typedef __bf16 bf16x8 __attribute__((ext_vector_type(8)));
typedef __bf16 bf16x2 __attribute__((ext_vector_type(2)));
typedef short s16x4 __attribute__((ext_vector_type(4)));
typedef float f32x2 __attribute__((ext_vector_type(2)));
typedef float f32x4 __attribute__((ext_vector_type(4)));
typedef float f32x16 __attribute__((ext_vector_type(16)));
typedef unsigned u32x2 __attribute__((ext_vector_type(2)));
typedef unsigned u32x4 __attribute__((ext_vector_type(4)));
typedef __amdgpu_buffer_rsrc_t rsrc_t;
#define DI __device__ __forceinline__
#define LAS __attribute__((address_space(3)))

#define CKV_WIN 8
#define CKV_NT 1

constexpr int DF = 1376;
constexpr int NFFN = 86;
constexpr int RFA = 16;
constexpr int RHB = 8;
constexpr unsigned WS_EPOCH = 0;
constexpr unsigned WS_QG = 4096;
constexpr unsigned WS_FA = WS_QG + 131072;
constexpr unsigned WS_QA = WS_FA + RFA * 2048;
constexpr unsigned WS_CLR = WS_QA + 131072;
constexpr unsigned WS_ML = WS_CLR;
constexpr unsigned WS_P2 = WS_ML + 262144;
constexpr unsigned WS_P4 = WS_P2 + 2097152;
constexpr unsigned WS_HB = WS_P4 + NFFN * 8192;
constexpr unsigned CLR_PER_WG = 12288;
static_assert(WS_HB + RHB * 8192 <= WS_CLR + 256 * CLR_PER_WG, "clear span");
constexpr unsigned WS_PART = WS_CLR + 256 * CLR_PER_WG;
constexpr unsigned WS_DBG = WS_PART + 8388608;
constexpr unsigned WS_END = WS_DBG + 256 * 8 * 16 * 8;
constexpr unsigned SENT = 0xFFFFFFFFu;
constexpr unsigned SPIN_CAP = 1u << 18;

constexpr unsigned L_Q = 131072;
constexpr unsigned L_QS = L_Q + 16384;
constexpr unsigned L_XN = L_Q + 17408;
constexpr unsigned L_P = L_Q;
constexpr unsigned L_MX = L_Q + 8192;
constexpr unsigned L_LS = L_Q + 9216;
constexpr unsigned L_OS = L_Q + 12288;
constexpr unsigned L_CC = L_Q + 28672;
constexpr unsigned L_CX = L_Q + 29696;
constexpr unsigned L_RED = L_Q + 8192;
constexpr unsigned L_HS = L_Q + 30720;
constexpr unsigned L_UV = 0;
constexpr unsigned L_GU = 16384;

#define STAMP(i) do {} while (0)
#define STAMP_DECL
#define STAMP_FLUSH

DI rsrc_t mk(const void* p, unsigned bytes) { return __builtin_amdgcn_make_buffer_rsrc((void*)p, 0, bytes, 0x00020000); }
DI u32x4 ld16(rsrc_t r, unsigned off) { return __builtin_amdgcn_raw_buffer_load_b128(r, off, 0, 0); }
DI u32x4 ld16c(rsrc_t r, unsigned off) { return __builtin_amdgcn_raw_buffer_load_b128(r, off, 0, 16); }
DI u32x2 ld8c(rsrc_t r, unsigned off) { return __builtin_amdgcn_raw_buffer_load_b64(r, off, 0, 16); }
DI void st16c(rsrc_t r, unsigned off, u32x4 v) { __builtin_amdgcn_raw_buffer_store_b128(v, r, off, 0, 16); }
DI void st8c(rsrc_t r, unsigned off, u32x2 v) { __builtin_amdgcn_raw_buffer_store_b64(v, r, off, 0, 16); }
DI void fence_c() { asm volatile("" ::: "memory"); }
DI void bar() { asm volatile("s_waitcnt lgkmcnt(0)\n\ts_barrier" ::: "memory"); }
DI void drain() { asm volatile("s_waitcnt vmcnt(0)" ::: "memory"); }
template <int N> DI void wait_vm() { asm volatile("s_waitcnt vmcnt(%0)" ::"n"(N) : "memory"); }
template <bool NT> DI void dma16(const void* sbase, unsigned voff, unsigned lds_dst) {
  unsigned keep;
  if (NT)
    asm volatile("s_mov_b32 %0, m0\n\ts_mov_b32 m0, %3\n\ts_nop 0\n\tglobal_load_lds_dwordx4 %1, %2 nt\n\ts_mov_b32 m0, %0"
                 : "=&s"(keep) : "v"(voff), "s"(sbase), "s"(lds_dst) : "memory");
  else
    asm volatile("s_mov_b32 %0, m0\n\ts_mov_b32 m0, %3\n\ts_nop 0\n\tglobal_load_lds_dwordx4 %1, %2\n\ts_mov_b32 m0, %0"
                 : "=&s"(keep) : "v"(voff), "s"(sbase), "s"(lds_dst) : "memory");
}
DI float bflo(unsigned u) { return __uint_as_float(u << 16); }
DI float bfhi(unsigned u) { return __uint_as_float(u & 0xffff0000u); }
DI unsigned pk(float a, float b) { f32x2 f = {a, b}; return __builtin_bit_cast(unsigned, __builtin_convertvector(f, bf16x2)); }
DI unsigned canon(float v) { unsigned u = __float_as_uint(v); return u == SENT ? 0x7fc00000u : u; }
template <int C> DI float dppf(float x) { return __int_as_float(__builtin_amdgcn_update_dpp(0, __float_as_int(x), C, 0xf, 0xf, true)); }
DI float sum16(float x) { auto r = __builtin_amdgcn_permlane16_swap(__float_as_uint(x), __float_as_uint(x), false, false); return __uint_as_float(r[0]) + __uint_as_float(r[1]); }
DI float sum32(float x) { auto r = __builtin_amdgcn_permlane32_swap(__float_as_uint(x), __float_as_uint(x), false, false); return __uint_as_float(r[0]) + __uint_as_float(r[1]); }
DI float max16(float x) { auto r = __builtin_amdgcn_permlane16_swap(__float_as_uint(x), __float_as_uint(x), false, false); return fmaxf(__uint_as_float(r[0]), __uint_as_float(r[1])); }
DI float max32(float x) { auto r = __builtin_amdgcn_permlane32_swap(__float_as_uint(x), __float_as_uint(x), false, false); return fmaxf(__uint_as_float(r[0]), __uint_as_float(r[1])); }
DI f32x4 mfma16(u32x4 a, u32x4 b, f32x4 c) { return __builtin_amdgcn_mfma_f32_16x16x32_bf16(__builtin_bit_cast(bf16x8, a), __builtin_bit_cast(bf16x8, b), c, 0, 0, 0); }
DI f32x16 mfma32(u32x4 a, u32x4 b, f32x16 c) { return __builtin_amdgcn_mfma_f32_32x32x16_bf16(__builtin_bit_cast(bf16x8, a), __builtin_bit_cast(bf16x8, b), c, 0, 0, 0); }
DI float sel4(f32x4 v, int g) { float a = (g & 1) ? v[1] : v[0], b = (g & 1) ? v[3] : v[2]; return (g & 2) ? b : a; }
DI bool bad4(u32x4 v) { return v[0] == SENT || v[1] == SENT || v[2] == SENT || v[3] == SENT; }
DI u32x4 colfrag(const u32x4* r, int j) {
  const unsigned sel = (j & 1) ? 0x07060302u : 0x05040100u;
  u32x4 o;
#pragma unroll
  for (int e = 0; e < 4; ++e) o[e] = __builtin_amdgcn_perm(r[2 * e + 1][j >> 1], r[2 * e][j >> 1], sel);
  return o;
}

__global__ void __launch_bounds__(512, 2)
fused_kernel(const uint16_t* __restrict__ x, const uint16_t* __restrict__ ckv, const uint16_t* __restrict__ wq,
             const uint16_t* __restrict__ wuk, const uint16_t* __restrict__ wuv, const uint16_t* __restrict__ wo,
             const uint16_t* __restrict__ wg, const uint16_t* __restrict__ wu, const uint16_t* __restrict__ wd,
             uint16_t* __restrict__ out, char* __restrict__ ws) {
  __shared__ __attribute__((aligned(16))) char lds[163840];
  LAS char* L = (LAS char*)lds;
  const unsigned lbase = (unsigned)(size_t)L;
  const int blk = blockIdx.x;
  const int lane = threadIdx.x & 63;
  const int wave = __builtin_amdgcn_readfirstlane(threadIdx.x >> 6);
  const int g = lane >> 4, n16 = lane & 15;
  const rsrc_t rws = mk(ws, WS_END);
  const int h = (blk & 7) * 4 + ((blk >> 3) & 3), sl = blk >> 5;
  const int ab = (blk & 7) >> 1, aj = (blk & 1) * 32 + (blk >> 3);
  const int fi = (blk & 7) * 11 + (blk >> 3);
  const bool ffn = (blk >> 3) < 11 && fi < NFFN;
#define LR16(off) (*(LAS u32x4*)(L + (off)))
#define TR(off) __builtin_bit_cast(u32x2, __builtin_amdgcn_ds_read_tr16_b64_v4i16((LAS s16x4*)(L + (off))))
  STAMP_DECL
  STAMP(0);

  if (wave < 4) {
    const int w = wave;
    bar();
    {
      const char* src = (const char*)ckv + ((size_t)(ab * 8192 + aj * 128 + w * 32) << 10);
#pragma unroll
      for (int r = 0; r < 32; ++r) {
        if (r >= CKV_WIN) wait_vm<CKV_WIN - 1>();
        const unsigned f = ((r & 3) << 2) | ((r >> 2) & 3);
        dma16<CKV_NT>(src, r * 1024 + ((lane ^ f) << 4), lbase + (w * 32 + r) * 1024);
      }
    }
    u32x4 gu[8], dn[8];
    if (ffn) {
      const rsrc_t rg = mk(wg, 512 * DF * 2), ru = mk(wu, 512 * DF * 2), rd = mk(wd, DF * 512 * 2);
#pragma unroll
      for (int i = 0; i < 4; ++i) {
        const unsigned o = (unsigned)((w * 128 + i * 32 + (lane >> 1)) * DF + fi * 16 + (lane & 1) * 8) * 2;
        gu[i] = ld16(rg, o); gu[4 + i] = ld16(ru, o);
      }
#pragma unroll
      for (int e = 0; e < 8; ++e) dn[e] = ld16(rd, (unsigned)((fi * 16 + (g & 1) * 8 + e) * 512 + w * 128 + n16 * 8) * 2);
      wait_vm<16>();
    } else {
      drain();
    }
    STAMP(1);
    bar();
    STAMP(2);
    const int r32 = lane & 31, hf = lane >> 5;
    f32x16 S;
#pragma unroll
    for (int i = 0; i < 16; ++i) S[i] = 0.f;
    {
      const unsigned f = ((r32 & 3) << 2) | ((r32 >> 2) & 3);
      const unsigned rowb = (w * 32 + r32) * 1024;
#pragma unroll
      for (int s = 0; s < 32; ++s) {
        const unsigned ch = 2 * s + hf;
        const u32x4 a = LR16(rowb + ((ch ^ f) << 4));
        const u32x4 b = LR16(L_Q + s * 1024 + lane * 16);
        S = mfma32(a, b, S);
      }
    }
    float mx = S[0];
#pragma unroll
    for (int i = 1; i < 16; ++i) mx = fmaxf(mx, S[i]);
    bar();
    *(LAS float*)(L + L_MX + r32 * 32 + (w * 2 + hf) * 4) = mx;
    bar();
    {
      const f32x4 a = *(LAS f32x4*)(L + L_MX + r32 * 32), b = *(LAS f32x4*)(L + L_MX + r32 * 32 + 16);
      mx = fmaxf(fmaxf(fmaxf(a[0], a[1]), fmaxf(a[2], a[3])), fmaxf(fmaxf(b[0], b[1]), fmaxf(b[2], b[3])));
    }
    {
      float ls = 0.f;
#pragma unroll
      for (int s = 0; s < 2; ++s) {
        u32x4 p;
#pragma unroll
        for (int e = 0; e < 4; ++e) {
          p[e] = pk(__builtin_amdgcn_exp2f(S[8 * s + 2 * e] - mx), __builtin_amdgcn_exp2f(S[8 * s + 2 * e + 1] - mx));
          ls += bflo(p[e]) + bfhi(p[e]);
        }
        LR16(L_P + (w * 2 + s) * 1024 + lane * 16) = p;
      }
      *(LAS float*)(L + L_LS + r32 * 32 + (w * 2 + hf) * 4) = ls;
    }
    bar();
    float lsum;
    {
      const f32x4 a = *(LAS f32x4*)(L + L_LS + r32 * 32), b = *(LAS f32x4*)(L + L_LS + r32 * 32 + 16);
      lsum = ((a[0] + a[1]) + (a[2] + a[3])) + ((b[0] + b[1]) + (b[2] + b[3]));
    }
    {
      u32x4 pf[8];
#pragma unroll
      for (int ks = 0; ks < 8; ++ks) pf[ks] = LR16(L_P + ks * 1024 + lane * 16);
      const int bk = (lane >> 4) & 1, q = (lane & 15) >> 2, p = lane & 3;
      const unsigned cl = 2 * (p & 1) + bk, c8 = (p >> 1) * 8;
#pragma unroll
      for (int pr = 0; pr < 2; ++pr) {
        f32x16 O[2];
#pragma unroll
        for (int t = 0; t < 2; ++t) {
#pragma unroll
          for (int i = 0; i < 16; ++i) O[t][i] = 0.f;
#pragma unroll
          for (int ks = 0; ks < 8; ++ks) {
            u32x4 a;
#pragma unroll
            for (int tt = 0; tt < 2; ++tt) {
              const unsigned row = 16 * ks + 8 * tt + 4 * hf + q;
              const unsigned f = (q << 2) | ((2 * tt + hf) & 3);
              const unsigned ch = (4 * (2 * pr + t) + cl) ^ f;
              const u32x2 v = TR(row * 1024 + w * 256 + ch * 16 + c8);
              a[2 * tt] = v[0]; a[2 * tt + 1] = v[1];
            }
            O[t] = mfma32(a, pf[ks], O[t]);
          }
        }
        const unsigned sb = L_OS + w * 4096;
#pragma unroll
        for (int t = 0; t < 2; ++t)
#pragma unroll
          for (int u = 0; u < 2; ++u) {
            u32x4 v;
#pragma unroll
            for (int e = 0; e < 4; ++e) v[e] = pk(O[t][8 * u + 2 * e], O[t][8 * u + 2 * e + 1]);
            LR16(sb + r32 * 128 + (((t * 4 + hf * 2 + u) ^ (r32 & 7)) << 4)) = v;
          }
#pragma unroll
        for (int i = 0; i < 4; ++i) {
          const int hd = 8 * i + (lane >> 3), c = lane & 7;
          const u32x4 v = LR16(sb + hd * 128 + ((c ^ (hd & 7)) << 4));
          st16c(rws, WS_PART + (unsigned)(((hd * 8 + 2 * w + pr) * 256 + ab * 64 + aj) * 128 + c * 16), v);
        }
      }
    }
    STAMP(3);
    bar();
    drain();
    if (lane < 32) { u32x2 v = {canon(mx), canon(lsum)}; st8c(rws, WS_ML + (unsigned)(((r32 * 4 + w) * 4 + ab) * 64 + aj) * 8, v); }
    STAMP(4);
    u32x4 gf[2][4], df[8];
    float csum[2];
    if (ffn) {
      const unsigned sb = L_GU + w * 8192;
#pragma unroll
      for (int i = 0; i < 8; ++i) LR16(sb + i * 1024 + lane * 16) = gu[i];
      const u32x4 ones = {0x3f803f80u, 0x3f803f80u, 0x3f803f80u, 0x3f803f80u};
#pragma unroll
      for (int m = 0; m < 2; ++m) {
        f32x4 c = {0.f, 0.f, 0.f, 0.f};
#pragma unroll
        for (int s = 0; s < 4; ++s) {
          const unsigned a = sb + m * 4096 + (32 * s + 8 * g + (n16 >> 2)) * 32 + (lane & 3) * 8;
          const u32x2 v0 = TR(a), v1 = TR(a + 128);
          gf[m][s] = u32x4{v0[0], v0[1], v1[0], v1[1]};
          c = mfma16(ones, gf[m][s], c);
        }
        csum[m] = c[0];
      }
#pragma unroll
      for (int j = 0; j < 8; ++j) df[j] = colfrag(dn, j);
    }
    u32x2 ml;
    for (unsigned it = 0; it < SPIN_CAP; ++it) {
      fence_c();
      ml = ld8c(rws, WS_ML + (unsigned)(((h * 4 + (sl >> 1)) * 4 + w) * 64 + lane) * 8);
      if (!__any(ml[0] == SENT || ml[1] == SENT)) break;
      __builtin_amdgcn_s_sleep(1);
    }
    fence_c();
    STAMP(5);
    u32x4 pv[8];
#pragma unroll
    for (int i = 0; i < 8; ++i)
      pv[i] = ld16c(rws, WS_PART + (unsigned)(((h * 8 + sl) * 256 + w * 64 + 8 * i + (lane >> 3)) * 128 + (lane & 7) * 16));
    {
      const float m = __uint_as_float(ml[0]);
      float M = m;
      M = fmaxf(M, dppf<0x128>(M)); M = fmaxf(M, dppf<0x124>(M)); M = fmaxf(M, dppf<0x122>(M)); M = fmaxf(M, dppf<0x121>(M));
      M = max16(M); M = max32(M);
      float wj = __builtin_amdgcn_exp2f(m - M);
      float l = wj * __uint_as_float(ml[1]);
      l += dppf<0x128>(l); l += dppf<0x124>(l); l += dppf<0x122>(l); l += dppf<0x121>(l);
      l = sum16(l); l = sum32(l);
      wj = wj / l;
      float acc[8];
#pragma unroll
      for (int e = 0; e < 8; ++e) acc[e] = 0.f;
#pragma unroll
      for (int i = 0; i < 8; ++i) {
        const float ww = __int_as_float(__builtin_amdgcn_ds_bpermute((8 * i + (lane >> 3)) * 4, __float_as_int(wj)));
#pragma unroll
        for (int e = 0; e < 4; ++e) { acc[2 * e] += ww * bflo(pv[i][e]); acc[2 * e + 1] += ww * bfhi(pv[i][e]); }
      }
#pragma unroll
      for (int e = 0; e < 8; ++e) { acc[e] += dppf<0x128>(acc[e]); acc[e] = sum32(sum16(acc[e])); }
      if (lane < 8) LR16(L_CC + w * 128 + lane * 16) = u32x4{pk(acc[0], acc[1]), pk(acc[2], acc[3]), pk(acc[4], acc[5]), pk(acc[6], acc[7])};
    }
    STAMP(6);
    bar();
    bar();
    f32x4 hres = {0.f, 0.f, 0.f, 0.f};
    if (w == 0) {
      const u32x2 xr = *(const u32x2*)(x + blk * 8 + (lane & 1) * 4);
      u32x4 v[8];
      for (unsigned it = 0; it < SPIN_CAP; ++it) {
        fence_c();
        bool bad = false;
#pragma unroll
        for (int i = 0; i < 8; ++i) v[i] = ld16c(rws, WS_P2 + (unsigned)((i * 32 + (lane >> 1)) * 8192 + blk * 32 + (lane & 1) * 16));
#pragma unroll
        for (int i = 0; i < 8; ++i) bad |= bad4(v[i]);
        if (!__any(bad)) break;
      }
      STAMP(7);
#pragma unroll
      for (int e = 0; e < 4; ++e) {
        float s = 0.f;
#pragma unroll
        for (int i = 0; i < 8; ++i) s += __uint_as_float(v[i][e]);
        s += dppf<0x128>(s); s += dppf<0x124>(s); s += dppf<0x122>(s);
        hres[e] = sum32(sum16(s));
      }
      hres[0] += bflo(xr[0]); hres[1] += bfhi(xr[0]); hres[2] += bflo(xr[1]); hres[3] += bfhi(xr[1]);
      if (lane < 2 * RHB)
        st16c(rws, WS_HB + (unsigned)((lane >> 1) * 8192 + blk * 32 + (lane & 1) * 16), u32x4{canon(hres[0]), canon(hres[1]), canon(hres[2]), canon(hres[3])});
      STAMP(8);
    }
    if (ffn) {
      u32x4 hv[8];
      for (unsigned it = 0; it < SPIN_CAP; ++it) {
        fence_c();
        bool bad = false;
#pragma unroll
        for (int i = 0; i < 8; ++i)
          hv[i] = ld16c(rws, WS_HB + (unsigned)((blk & (RHB - 1)) * 8192 + ((lane & 3) * 512 + w * 128 + (i >> 1) * 32 + g * 8 + (i & 1) * 4) * 4));
#pragma unroll
        for (int i = 0; i < 8; ++i) bad |= bad4(hv[i]);
        if (!__any(bad)) break;
      }
      STAMP(9);
      f32x4 G = {0.f, 0.f, 0.f, 0.f}, U = {0.f, 0.f, 0.f, 0.f};
      float s1 = 0.f, s2 = 0.f;
#pragma unroll
      for (int s = 0; s < 4; ++s) {
        u32x4 a;
#pragma unroll
        for (int e = 0; e < 4; ++e) {
          a[e] = pk(__uint_as_float(hv[2 * s + (e >> 1)][2 * (e & 1)]), __uint_as_float(hv[2 * s + (e >> 1)][2 * (e & 1) + 1]));
          const float lo = bflo(a[e]), hi = bfhi(a[e]);
          s1 += lo + hi; s2 += lo * lo + hi * hi;
        }
        G = mfma16(a, gf[0][s], G); U = mfma16(a, gf[1][s], U);
      }
      s1 = sum32(sum16(s1)); s2 = sum32(sum16(s2));
      LAS float* red = (LAS float*)(L + L_RED + w * 704);
      red[g * 16 + n16] = sel4(G, g);
      red[64 + g * 16 + n16] = sel4(U, g);
      if (g < 2) red[128 + g * 16 + n16] = g ? csum[1] : csum[0];
      if (lane < 4) { red[160 + lane * 2] = s1; red[161 + lane * 2] = s2; }
    }
    bar();
    if (ffn) {
      float G = 0.f, U = 0.f, cg = 0.f, cu = 0.f, s1 = 0.f, s2 = 0.f;
#pragma unroll
      for (int k = 0; k < 4; ++k) {
        LAS float* red = (LAS float*)(L + L_RED + k * 704);
        G += red[lane]; U += red[64 + lane]; cg += red[128 + n16]; cu += red[144 + n16];
        s1 += red[160 + g * 2]; s2 += red[161 + g * 2];
      }
      const float mean = s1 * (1.f / 512.f);
      const float var = fmaxf(s2 * (1.f / 512.f) - mean * mean, 0.f);
      const float rstd = 1.f / sqrtf(var + 1e-5f);
      const float gg = (G - mean * cg) * rstd, uu = (U - mean * cu) * rstd;
      const float z = 0.7978845608028654f * (gg + 0.044715f * gg * gg * gg);
      const float th = 1.f - 2.f / (1.f + __expf(2.f * z));
      const float hh = 0.5f * gg * (1.f + th) * uu;
      *(LAS uint16_t*)(L + L_HS + w * 128 + lane * 2) = (uint16_t)pk(hh, 0.f);
      fence_c();
      u32x4 a = LR16(L_HS + w * 128 + (lane & 3) * 32 + (g & 1) * 16);
      if (g >= 2) a = u32x4{0u, 0u, 0u, 0u};
      f32x4 D[8];
#pragma unroll
      for (int j = 0; j < 8; ++j) D[j] = mfma16(a, df[j], f32x4{0.f, 0.f, 0.f, 0.f});
#pragma unroll
      for (int i = 0; i < 2; ++i) {
        u32x4 v;
#pragma unroll
        for (int e = 0; e < 4; ++e) {
          const float lo = (g & 2) ? D[e][2 * i + 1] : D[e][2 * i], hi = (g & 2) ? D[4 + e][2 * i + 1] : D[4 + e][2 * i];
          v[e] = canon((g & 1) ? hi : lo);
        }
        st16c(rws, WS_P4 + (unsigned)(fi * 8192 + ((2 * i + (g >> 1)) * 512 + w * 128 + n16 * 8 + (g & 1) * 4) * 4), v);
      }
      STAMP(10);
    }
    if (w == 0) {
      u32x4 v[3];
      const bool last = (lane >> 1) < NFFN - 64;
      for (unsigned it = 0; it < SPIN_CAP; ++it) {
        fence_c();
        bool bad = false;
#pragma unroll
        for (int i = 0; i < 3; ++i) {
          const int p = (i < 2 || last) ? i * 32 + (lane >> 1) : 0;
          v[i] = ld16c(rws, WS_P4 + (unsigned)(p * 8192 + blk * 32 + (lane & 1) * 16));
        }
#pragma unroll
        for (int i = 0; i < 3; ++i) bad |= bad4(v[i]);
        if (!__any(bad)) break;
      }
      STAMP(11);
      float o[4];
#pragma unroll
      for (int e = 0; e < 4; ++e) {
        float s = __uint_as_float(v[0][e]) + __uint_as_float(v[1][e]) + (last ? __uint_as_float(v[2][e]) : 0.f);
        s += dppf<0x128>(s); s += dppf<0x124>(s); s += dppf<0x122>(s);
        o[e] = sum32(sum16(s)) + hres[e];
      }
      if (lane < 2) *(u32x2*)(out + blk * 8 + lane * 4) = u32x2{pk(o[0], o[1]), pk(o[2], o[3])};
      STAMP(12);
    }
    STAMP_FLUSH;
  } else {
    const int a = wave - 4;
    volatile LAS unsigned* goflag = (volatile LAS unsigned*)(L + L_Q + (28 + a) * 1024);
    if (a == 0) {
      const u32x2 ep = ld8c(rws, WS_EPOCH);
      const rsrc_t rk = mk(wuk, 512 * 4096 * 2);
      u32x4 xr[4], wk[2][2][4];
      fence_c();
#pragma unroll
      for (int i = 0; i < 4; ++i)
        asm volatile("global_load_dwordx4 %0, %1, %2" : "=v"(xr[i]) : "v"((unsigned)(lane * 64 + i * 16)), "s"(x) : "memory");
#pragma unroll
      for (int i = 0; i < 16; ++i)
        dma16<false>(wq, (unsigned)((32 * i + (lane >> 1)) * 4096 + h * 128 + sl * 16 + (lane & 1) * 8) * 2, lbase + L_Q + i * 1024);
#pragma unroll
      for (int pr = 0; pr < 2; ++pr)
#pragma unroll
        for (int t = 0; t < 2; ++t)
#pragma unroll
          for (int s = 0; s < 4; ++s)
            wk[pr][t][s] = ld16(rk, (unsigned)((64 * sl + 32 * pr + 8 * (n16 >> 2) + 4 * t + (n16 & 3)) * 4096 + h * 128 + 32 * s + 8 * g) * 2);
      fence_c();
      bar();
#pragma unroll
      for (int i = 0; i < 12; ++i) st16c(rws, WS_CLR + blk * CLR_PER_WG + i * 1024 + lane * 16, u32x4{SENT, SENT, SENT, SENT});
      fence_c();
      asm volatile("s_waitcnt vmcnt(44)" : "+v"(xr[0]), "+v"(xr[1]), "+v"(xr[2]), "+v"(xr[3])::"memory");
      float s1 = 0.f;
#pragma unroll
      for (int i = 0; i < 4; ++i)
#pragma unroll
        for (int e = 0; e < 4; ++e) s1 += bflo(xr[i][e]) + bfhi(xr[i][e]);
      s1 += dppf<0x128>(s1); s1 += dppf<0x124>(s1); s1 += dppf<0x122>(s1); s1 += dppf<0x121>(s1);
      const float mean = s1 * (1.f / 512.f);
      float s2 = 0.f;
#pragma unroll
      for (int i = 0; i < 4; ++i)
#pragma unroll
        for (int e = 0; e < 4; ++e) { const float lo = bflo(xr[i][e]) - mean, hi = bfhi(xr[i][e]) - mean; s2 += lo * lo + hi * hi; }
      s2 += dppf<0x128>(s2); s2 += dppf<0x124>(s2); s2 += dppf<0x122>(s2); s2 += dppf<0x121>(s2);
      const float rstd = 1.f / sqrtf(s2 * (1.f / 512.f) + 1e-5f);
#pragma unroll
      for (int i = 0; i < 4; ++i) {
        u32x4 v;
#pragma unroll
        for (int e = 0; e < 4; ++e) v[e] = pk((bflo(xr[i][e]) - mean) * rstd, (bfhi(xr[i][e]) - mean) * rstd);
        LR16(L_XN + g * 1040 + n16 * 64 + i * 16) = v;
      }
      u32x4 xa[16];
#pragma unroll
      for (int ks = 0; ks < 16; ++ks) xa[ks] = LR16(L_XN + (lane & 3) * 1040 + ks * 64 + g * 16);
      wait_vm<28>();
      STAMP(1);
      f32x4 qa[2] = {{0.f, 0.f, 0.f, 0.f}, {0.f, 0.f, 0.f, 0.f}};
#pragma unroll
      for (int ks = 0; ks < 16; ++ks) {
        const unsigned ad = L_Q + (32 * ks + 8 * g + (n16 >> 2)) * 32 + (lane & 3) * 8;
        const u32x2 v0 = TR(ad), v1 = TR(ad + 128);
        qa[ks & 1] = mfma16(xa[ks], u32x4{v0[0], v0[1], v1[0], v1[1]}, qa[ks & 1]);
      }
      const unsigned long long tag = (((unsigned long long)ep[1] << 32) | ep[0]) + 1ull;
      const unsigned tg_lo = (unsigned)tag << 16, tg_hi = (unsigned)(tag >> 16);
      {
        const float v = sel4(qa[0] + qa[1], g);
        st8c(rws, WS_QG + (unsigned)((h * 4 + g) * 128 + sl * 16 + n16) * 8, u32x2{tg_lo | (pk(v, 0.f) & 0xffffu), tg_hi});
      }
      STAMP(2);
      u32x4 qg[4];
      for (unsigned it = 0; it < SPIN_CAP; ++it) {
        fence_c();
        bool bad = false;
#pragma unroll
        for (int i = 0; i < 4; ++i) qg[i] = ld16c(rws, WS_QG + (unsigned)((h * 4 + g) * 128 + n16 * 8) * 8 + i * 16);
#pragma unroll
        for (int i = 0; i < 4; ++i)
          bad |= (qg[i][1] != tg_hi) | (qg[i][3] != tg_hi) | ((qg[i][0] & 0xffff0000u) != tg_lo) | ((qg[i][2] & 0xffff0000u) != tg_lo);
        if (!__any(bad)) break;
      }
      STAMP(3);
      {
        u32x4 v;
#pragma unroll
        for (int i = 0; i < 4; ++i) v[i] = __builtin_amdgcn_perm(qg[i][2], qg[i][0], 0x05040100u);
        LR16(L_QS + g * 256 + n16 * 16) = v;
      }
      u32x4 qf[4];
#pragma unroll
      for (int s = 0; s < 4; ++s) qf[s] = LR16(L_QS + (lane & 3) * 256 + (32 * s + 8 * g) * 2);
      f32x4 d[2][2];
#pragma unroll
      for (int pr = 0; pr < 2; ++pr)
#pragma unroll
        for (int t = 0; t < 2; ++t) {
          d[pr][t] = f32x4{0.f, 0.f, 0.f, 0.f};
#pragma unroll
          for (int s = 0; s < 4; ++s) d[pr][t] = mfma16(wk[pr][t][s], qf[s], d[pr][t]);
        }
      {
        const int dd = n16 >> 2;
        const float sc = 0.08838834764831845f * 1.4426950408889634f;
        const f32x4 lo = (dd & 1) ? d[1][0] : d[0][0], hi = (dd & 1) ? d[1][1] : d[0][1];
        const u32x4 v = {pk(lo[0] * sc, lo[1] * sc), pk(lo[2] * sc, lo[3] * sc), pk(hi[0] * sc, hi[1] * sc), pk(hi[2] * sc, hi[3] * sc)};
        if (dd < 2) st16c(rws, WS_QA + (unsigned)((lane & 3) * 32768 + (4 * sl + 2 * dd + (g >> 1)) * 1024 + ((g & 1) * 32 + h) * 16), v);
      }
      drain();
      const u32x2 tg = {(unsigned)tag, (unsigned)(tag >> 32)};
      if (lane < RFA) st8c(rws, WS_FA + lane * 2048 + blk * 8, tg);
      STAMP(4);
      for (unsigned it = 0; it < SPIN_CAP; ++it) {
        fence_c();
        const u32x4 f0 = ld16c(rws, WS_FA + (blk >> 4) * 2048 + lane * 16), f1 = ld16c(rws, WS_FA + (blk >> 4) * 2048 + 1024 + lane * 16);
        const bool bad = (f0[0] != tg[0]) | (f0[1] != tg[1]) | (f0[2] != tg[0]) | (f0[3] != tg[1]) |
                         (f1[0] != tg[0]) | (f1[1] != tg[1]) | (f1[2] != tg[0]) | (f1[3] != tg[1]);
        if (!__any(bad)) break;
      }
      fence_c();
      if (lane < 3) *(volatile LAS unsigned*)(L + L_Q + (29 + lane) * 1024) = 1u;
      if (blk == 0 && lane == 0) st8c(rws, WS_EPOCH, tg);
      STAMP(5);
    } else {
      if (lane == 0) *goflag = 0u;
      bar();
      for (unsigned it = 0; it < SPIN_CAP * 16; ++it) {
        if (*goflag != 0u) break;
        __builtin_amdgcn_s_sleep(2);
      }
    }
    fence_c();
    u32x4 qv[8];
#pragma unroll
    for (int i = 0; i < 8; ++i) qv[i] = ld16c(rws, WS_QA + (unsigned)(ab * 32768 + (4 * i + a) * 1024 + lane * 16));
    fence_c();
    u32x4 uv[4], wor[4][8];
    {
      const rsrc_t rv = mk(wuv, 512 * 4096 * 2), ro = mk(wo, 4096 * 512 * 2);
#pragma unroll
      for (int i = 0; i < 4; ++i) uv[i] = ld16(rv, (unsigned)((64 * sl + 16 * i + (lane >> 2)) * 4096 + h * 128 + 32 * a + (lane & 3) * 8) * 2);
#pragma unroll
      for (int s = 0; s < 4; ++s)
#pragma unroll
        for (int e = 0; e < 8; ++e) wor[s][e] = ld16(ro, (unsigned)((h * 128 + 32 * s + 8 * g + e) * 512 + 128 * a + n16 * 8) * 2);
    }
    fence_c();
#pragma unroll
    for (int i = 0; i < 8; ++i) LR16(L_Q + (4 * i + a) * 1024 + lane * 16) = qv[i];
    STAMP(6);
    bar();
    bar();
    bar();
    bar();
    bar();
    u32x4 vf[2][2];
    {
      const unsigned sb = L_UV + a * 4096;
#pragma unroll
      for (int i = 0; i < 4; ++i) LR16(sb + i * 1024 + lane * 16) = uv[i];
#pragma unroll
      for (int tt = 0; tt < 2; ++tt)
#pragma unroll
        for (int s = 0; s < 2; ++s) {
          const unsigned ad = sb + (32 * s + 8 * g + (n16 >> 2)) * 64 + tt * 32 + (lane & 3) * 8;
          const u32x2 v0 = TR(ad), v1 = TR(ad + 256);
          vf[tt][s] = u32x4{v0[0], v0[1], v1[0], v1[1]};
        }
    }
    u32x4 of[4][8];
#pragma unroll
    for (int s = 0; s < 4; ++s)
#pragma unroll
      for (int j = 0; j < 8; ++j) of[s][j] = colfrag(wor[s], j);
#pragma unroll
    for (int s = 0; s < 4; ++s)
#pragma unroll
      for (int j = 0; j < 8; ++j) asm volatile("" : "+v"(of[s][j]));
    STAMP(7);
    bar();
    {
      const u32x4 c0 = LR16(L_CC + (lane & 3) * 128 + g * 16), c1 = LR16(L_CC + (lane & 3) * 128 + 64 + g * 16);
      f32x4 d0 = mfma16(vf[0][0], c0, f32x4{0.f, 0.f, 0.f, 0.f}), d1 = mfma16(vf[1][0], c0, f32x4{0.f, 0.f, 0.f, 0.f});
      d0 = mfma16(vf[0][1], c1, d0); d1 = mfma16(vf[1][1], c1, d1);
      const int dd = n16 >> 2;
      const f32x4 d = (dd & 1) ? d1 : d0;
      if (dd < 2) *(LAS u32x2*)(L + L_CX + (lane & 3) * 256 + (32 * a + 16 * dd + 4 * g) * 2) = u32x2{pk(d[0], d[1]), pk(d[2], d[3])};
    }
    bar();
    {
      u32x4 cf[4];
#pragma unroll
      for (int s = 0; s < 4; ++s) cf[s] = LR16(L_CX + (lane & 3) * 256 + (32 * s + 8 * g) * 2);
      f32x4 D[8];
#pragma unroll
      for (int j = 0; j < 8; ++j) D[j] = f32x4{0.f, 0.f, 0.f, 0.f};
#pragma unroll
      for (int s = 0; s < 4; ++s)
#pragma unroll
        for (int j = 0; j < 8; ++j) D[j] = mfma16(cf[s], of[s][j], D[j]);
#pragma unroll
      for (int i = 0; i < 2; ++i) {
        u32x4 v;
#pragma unroll
        for (int e = 0; e < 4; ++e) {
          const float lo = (g & 2) ? D[e][2 * i + 1] : D[e][2 * i], hi = (g & 2) ? D[4 + e][2 * i + 1] : D[4 + e][2 * i];
          v[e] = canon((g & 1) ? hi : lo);
        }
        st16c(rws, WS_P2 + (unsigned)(blk * 8192 + ((2 * i + (g >> 1)) * 512 + 128 * a + n16 * 8 + (g & 1) * 4) * 4), v);
      }
    }
    STAMP(8);
    bar();
    STAMP_FLUSH;
  }
}

extern "C" void kernel_launch(void* const* d_in, const int* in_sizes, int n_in, void* d_out, int out_size, void* d_ws,
                              size_t ws_size, hipStream_t stream) {
  fused_kernel<<<256, 512, 0, stream>>>((const uint16_t*)d_in[0], (const uint16_t*)d_in[1], (const uint16_t*)d_in[2],
                                        (const uint16_t*)d_in[3], (const uint16_t*)d_in[4], (const uint16_t*)d_in[5],
                                        (const uint16_t*)d_in[7], (const uint16_t*)d_in[8], (const uint16_t*)d_in[9],
                                        (uint16_t*)d_out, (char*)d_ws);
}
```

```cpp
#include <hip/hip_runtime.h>
#include <hip/hip_bf16.h>
#include <stdint.h>

typedef __bf16 bf16x8 __attribute__((ext_vector_type(8)));
typedef __bf16 bf16x2 __attribute__((ext_vector_type(2)));
typedef short s16x4 __attribute__((ext_vector_type(4)));
typedef float f32x2 __attribute__((ext_vector_type(2)));
typedef float f32x4 __attribute__((ext_vector_type(4)));
typedef float f32x16 __attribute__((ext_vector_type(16)));
typedef unsigned u32x2 __attribute__((ext_vector_type(2)));
typedef unsigned u32x4 __attribute__((ext_vector_type(4)));
typedef __amdgpu_buffer_rsrc_t rsrc_t;
#define DI __device__ __forceinline__
#define LAS __attribute__((address_space(3)))

#define CKV_WIN 6
#define WPOL 0
#define TPOL ""
#define QA_SLEEP 24
#define WQ_B0 31
#define CKV_PRE 0
#define TOUCH_AT 16
#define ML_SLEEP 2

constexpr int DF = 1376;
constexpr int NFFN = 86;
constexpr unsigned PST = 32768 + 256;
constexpr unsigned WS_EPOCH = 0;
constexpr unsigned WS_QA = 4096;
constexpr unsigned WS_CLR = WS_QA + 262144;
constexpr unsigned WS_ML = WS_CLR;
constexpr unsigned WS_HB = WS_ML + 65536;
constexpr unsigned WS_CX = WS_HB + 32768;
constexpr unsigned WS_P4 = WS_CX + 262144;
constexpr unsigned WS_P2 = WS_P4 + 32 * NFFN * 128;
constexpr unsigned CLR_PER_WG = 7168;
static_assert(WS_P2 + 32 * PST <= WS_CLR + 256 * CLR_PER_WG, "clear span");
constexpr unsigned WS_PART = WS_CLR + 256 * CLR_PER_WG;
constexpr unsigned WS_DBG = WS_PART + 256 * PST;
constexpr unsigned WS_END = WS_DBG + 256 * 8 * 16 * 8;
constexpr unsigned SENT = 0xFFFFFFFFu;
constexpr unsigned SPIN_CAP = 1u << 18;

constexpr unsigned L_Q = 131072;
constexpr unsigned L_WK = L_Q;
constexpr unsigned L_QP = L_Q + 16384;
constexpr unsigned L_XN = L_Q + 21504;
constexpr unsigned L_SY = L_Q + 28 * 1024;
constexpr unsigned L_P = L_Q;
constexpr unsigned L_MX = L_Q + 8192;
constexpr unsigned L_LS = L_Q + 9216;
constexpr unsigned L_OS = L_Q + 12288;
constexpr unsigned L_CC = L_Q + 28672;
constexpr unsigned L_RED = L_Q + 8192;
constexpr unsigned L_HS = L_Q + 30720;
constexpr unsigned L_UV = 0;
constexpr unsigned L_GU = 32768;
constexpr unsigned L_RS = 65536;

#define STAMP(i) do {} while (0)
#define STAMP_DECL
#define STAMP_FLUSH

DI rsrc_t mk(const void* p, unsigned bytes) { return __builtin_amdgcn_make_buffer_rsrc((void*)p, 0, bytes, 0x00020000); }
DI u32x4 ld16(rsrc_t r, unsigned off) { return __builtin_amdgcn_raw_buffer_load_b128(r, off, 0, WPOL); }
DI u32x4 ld16n(rsrc_t r, unsigned off) { return __builtin_amdgcn_raw_buffer_load_b128(r, off, 0, 2); }
DI u32x4 ld16c(rsrc_t r, unsigned off) { return __builtin_amdgcn_raw_buffer_load_b128(r, off, 0, 16); }
DI u32x2 ld8c(rsrc_t r, unsigned off) { return __builtin_amdgcn_raw_buffer_load_b64(r, off, 0, 16); }
DI void st16c(rsrc_t r, unsigned off, u32x4 v) { __builtin_amdgcn_raw_buffer_store_b128(v, r, off, 0, 16); }
DI void st8c(rsrc_t r, unsigned off, u32x2 v) { __builtin_amdgcn_raw_buffer_store_b64(v, r, off, 0, 16); }
DI void fence_c() { asm volatile("" ::: "memory"); }
DI void bar() { asm volatile("s_waitcnt lgkmcnt(0)\n\ts_barrier" ::: "memory"); }
DI void drain() { asm volatile("s_waitcnt vmcnt(0)" ::: "memory"); }
template <int N> DI void wait_vm() { asm volatile("s_waitcnt vmcnt(%0)" ::"n"(N) : "memory"); }
template <bool NT> DI void dma16(const void* sbase, unsigned voff, unsigned lds_dst) {
  unsigned keep;
  if (NT)
    asm volatile("s_mov_b32 %0, m0\n\ts_mov_b32 m0, %3\n\ts_nop 0\n\tglobal_load_lds_dwordx4 %1, %2 nt\n\ts_mov_b32 m0, %0"
                 : "=&s"(keep) : "v"(voff), "s"(sbase), "s"(lds_dst) : "memory");
  else
    asm volatile("s_mov_b32 %0, m0\n\ts_mov_b32 m0, %3\n\ts_nop 0\n\tglobal_load_lds_dwordx4 %1, %2\n\ts_mov_b32 m0, %0"
                 : "=&s"(keep) : "v"(voff), "s"(sbase), "s"(lds_dst) : "memory");
}
DI float bflo(unsigned u) { return __uint_as_float(u << 16); }
DI float bfhi(unsigned u) { return __uint_as_float(u & 0xffff0000u); }
DI unsigned pk(float a, float b) { f32x2 f = {a, b}; return __builtin_bit_cast(unsigned, __builtin_convertvector(f, bf16x2)); }
DI unsigned canon(float v) { unsigned u = __float_as_uint(v); return u == SENT ? 0x7fc00000u : u; }
DI unsigned canon2(unsigned u) { return u == SENT ? 0x7fc07fc0u : u; }
DI void sb0() { __builtin_amdgcn_sched_barrier(0); }
template <int C> DI float dppf(float x) { return __int_as_float(__builtin_amdgcn_update_dpp(0, __float_as_int(x), C, 0xf, 0xf, true)); }
DI float sum16(float x) { auto r = __builtin_amdgcn_permlane16_swap(__float_as_uint(x), __float_as_uint(x), false, false); return __uint_as_float(r[0]) + __uint_as_float(r[1]); }
DI float sum32(float x) { auto r = __builtin_amdgcn_permlane32_swap(__float_as_uint(x), __float_as_uint(x), false, false); return __uint_as_float(r[0]) + __uint_as_float(r[1]); }
DI float max16(float x) { auto r = __builtin_amdgcn_permlane16_swap(__float_as_uint(x), __float_as_uint(x), false, false); return fmaxf(__uint_as_float(r[0]), __uint_as_float(r[1])); }
DI float max32(float x) { auto r = __builtin_amdgcn_permlane32_swap(__float_as_uint(x), __float_as_uint(x), false, false); return fmaxf(__uint_as_float(r[0]), __uint_as_float(r[1])); }
DI f32x4 mfma16(u32x4 a, u32x4 b, f32x4 c) { return __builtin_amdgcn_mfma_f32_16x16x32_bf16(__builtin_bit_cast(bf16x8, a), __builtin_bit_cast(bf16x8, b), c, 0, 0, 0); }
DI f32x16 mfma32(u32x4 a, u32x4 b, f32x16 c) { return __builtin_amdgcn_mfma_f32_32x32x16_bf16(__builtin_bit_cast(bf16x8, a), __builtin_bit_cast(bf16x8, b), c, 0, 0, 0); }
DI float sel4(f32x4 v, int g) { float a = (g & 1) ? v[1] : v[0], b = (g & 1) ? v[3] : v[2]; return (g & 2) ? b : a; }
DI bool bad4(u32x4 v) { return v[0] == SENT || v[1] == SENT || v[2] == SENT || v[3] == SENT; }
DI u32x4 colfrag(const u32x4* r, int j) {
  const unsigned sel = (j & 1) ? 0x07060302u : 0x05040100u;
  u32x4 o;
#pragma unroll
  for (int e = 0; e < 4; ++e) o[e] = __builtin_amdgcn_perm(r[2 * e + 1][j >> 1], r[2 * e][j >> 1], sel);
  return o;
}

DI unsigned max4u(unsigned m, u32x4 v) { return max(max(max(m, v[0]), v[1]), max(v[2], v[3])); }
DI u32x4 pick(const u32x4 msk, unsigned ww) { return u32x4{msk[0] & ww, msk[1] & ww, msk[2] & ww, msk[3] & ww}; }
DI f32x4 fold(f32x4 d) {
#pragma unroll
  for (int r = 0; r < 4; ++r) {
    const unsigned t = __float_as_uint(dppf<0x128>(d[r]));
    d[r] += __uint_as_float(__builtin_amdgcn_permlane32_swap(t, t, false, false)[1]);
  }
  return d;
}
template <int N> DI f32x4 line_sum(rsrc_t rws, unsigned base, int first, int count, int lane, const u32x4 msk) {
  u32x4 v[N], one[N];
  unsigned off[N];
#pragma unroll
  for (int i = 0; i < N; ++i) {
    const int p = first + 8 * i + (lane >> 3);
    off[i] = base + (unsigned)((p < count ? p : 0) * 128 + (lane & 7) * 16);
    one[i] = pick(msk, p < count ? 0x3f803f80u : 0u);
  }
  for (unsigned it = 0; it < SPIN_CAP; ++it) {
    fence_c();
#pragma unroll
    for (int i = 0; i < N; ++i) v[i] = ld16c(rws, off[i]);
    unsigned m = 0;
#pragma unroll
    for (int i = 0; i < N; ++i) m = max4u(m, v[i]);
    if (!__any(m == SENT)) break;
    __builtin_amdgcn_s_sleep(1);
  }
  f32x4 d[2] = {{0.f, 0.f, 0.f, 0.f}, {0.f, 0.f, 0.f, 0.f}};
#pragma unroll
  for (int i = 0; i < N; ++i) d[i & 1] = mfma16(one[i], v[i], d[i & 1]);
  return fold(d[0] + d[1]);
}

__global__ void __launch_bounds__(512, 2)
fused_kernel(const uint16_t* __restrict__ x, const uint16_t* __restrict__ ckv, const uint16_t* __restrict__ wq,
             const uint16_t* __restrict__ wuk, const uint16_t* __restrict__ wuv, const uint16_t* __restrict__ wo,
             const uint16_t* __restrict__ wg, const uint16_t* __restrict__ wu, const uint16_t* __restrict__ wd,
             uint16_t* __restrict__ out, char* __restrict__ ws) {
  __shared__ __attribute__((aligned(16))) char lds[163840];
  LAS char* L = (LAS char*)lds;
  const unsigned lbase = (unsigned)(size_t)L;
  const int blk = blockIdx.x;
  const int lane = threadIdx.x & 63;
  const int wave = __builtin_amdgcn_readfirstlane(threadIdx.x >> 6);
  const int g = lane >> 4, n16 = lane & 15;
  const rsrc_t rws = mk(ws, WS_END);
  const int h = (blk & 7) * 4 + ((blk >> 3) & 3), sl = blk >> 5;
  const int ab = (blk & 7) >> 1, aj = (blk & 1) * 32 + (blk >> 3);
  const int fi = (blk & 7) * 11 + (blk >> 3);
  const bool ffn = (blk >> 3) < 11 && fi < NFFN;
#define LR16(off) (*(LAS u32x4*)(L + (off)))
#define TR(off) __builtin_bit_cast(u32x2, __builtin_amdgcn_ds_read_tr16_b64_v4i16((LAS s16x4*)(L + (off))))
  STAMP_DECL
  STAMP(0);

  if (wave < 4) {
    const int w = wave;
    unsigned touch = 0;
    if (w == 0) {
#pragma unroll
      for (int i = 0; i < 7; ++i) st16c(rws, WS_CLR + blk * CLR_PER_WG + i * 1024 + lane * 16, u32x4{SENT, SENT, SENT, SENT});
    } else if (w == 1) {
      u32x4 xr[4];
#pragma unroll
      for (int i = 0; i < 4; ++i) xr[i] = *(const u32x4*)(x + lane * 32 + i * 8);
      float s1 = 0.f;
#pragma unroll
      for (int i = 0; i < 4; ++i)
#pragma unroll
        for (int e = 0; e < 4; ++e) s1 += bflo(xr[i][e]) + bfhi(xr[i][e]);
      s1 += dppf<0x128>(s1); s1 += dppf<0x124>(s1); s1 += dppf<0x122>(s1); s1 += dppf<0x121>(s1);
      const float mean = s1 * (1.f / 512.f);
      float s2 = 0.f;
#pragma unroll
      for (int i = 0; i < 4; ++i)
#pragma unroll
        for (int e = 0; e < 4; ++e) { const float lo = bflo(xr[i][e]) - mean, hi = bfhi(xr[i][e]) - mean; s2 += lo * lo + hi * hi; }
      s2 += dppf<0x128>(s2); s2 += dppf<0x124>(s2); s2 += dppf<0x122>(s2); s2 += dppf<0x121>(s2);
      const float rstd = 1.f / sqrtf(s2 * (1.f / 512.f) + 1e-5f);
#pragma unroll
      for (int i = 0; i < 4; ++i) {
        u32x4 v;
#pragma unroll
        for (int e = 0; e < 4; ++e) v[e] = pk((bflo(xr[i][e]) - mean) * rstd, (bfhi(xr[i][e]) - mean) * rstd);
        LR16(L_XN + g * 1040 + n16 * 64 + i * 16) = v;
      }
    }
    {
      const char* src = (const char*)ckv + ((size_t)(ab * 8192 + aj * 128 + w * 32) << 10);
      const int xc = blk & 7, id = blk >> 3;
      const unsigned og = (unsigned)((id * 16 + (lane >> 2)) * (DF * 2) + xc * 352 + (lane & 3) * 116);
      const unsigned od = (unsigned)(xc * 176 * 1024 + (id * 44 + (w & 1) * 22 + min(lane, 21)) * 128);
      const char* p0 = (const char*)(w == 0 ? wg : w == 1 ? wu : wd) + min(w < 2 ? og : od, 512u * DF * 2 - 4u);
#pragma unroll
      for (int r = 0; r < 32; ++r) {
        if (r == CKV_PRE) { bar(); STAMP(12); }
        if (r == CKV_WIN && w == 0) { wait_vm<CKV_WIN>(); if (lane < 2) *(volatile LAS unsigned*)(L + L_SY + lane * 1024 + 24) = 1u; }
        if (r >= CKV_WIN) wait_vm<CKV_WIN - 1>();
        if (r == TOUCH_AT) asm volatile("global_load_dword %0, %1, off" TPOL : "+v"(touch) : "v"(p0) : "memory");
        const unsigned f = ((r & 3) << 2) | ((r >> 2) & 3);
        dma16<true>(src, r * 1024 + ((lane ^ f) << 4), lbase + (w * 32 + r) * 1024);
      }
    }
    const int r32 = lane & 31, hf = lane >> 5;
    unsigned qo[8], to[4][2][2];
    u32x4 msk;
    {
      const unsigned fr = ((r32 & 3) << 2) | ((r32 >> 2) & 3);
#pragma unroll
      for (int s = 0; s < 8; ++s) qo[s] = (w * 32 + r32) * 1024 + (((2 * s + hf) ^ fr) << 4);
      const int bk = (lane >> 4) & 1, q = (lane & 15) >> 2, p = lane & 3;
      const unsigned cl = 2 * (p & 1) + bk, c8 = (p >> 1) * 8;
#pragma unroll
      for (int k = 0; k < 4; ++k)
#pragma unroll
        for (int tt = 0; tt < 2; ++tt)
#pragma unroll
          for (int u = 0; u < 2; ++u) {
            to[k][tt][u] = (64 * u + 8 * tt + 4 * hf + q) * 1024 + w * 256 + (((4 * k + cl) ^ ((q << 2) | ((2 * tt + hf) & 3))) << 4) + c8;
            asm volatile("" : "+v"(to[k][tt][u]));
          }
    }
    drain();
    asm volatile("" : "+v"(touch));
    STAMP(1);
    u32x4 ca[32];
#pragma unroll
    for (int s = 0; s < 32; ++s) ca[s] = LR16(qo[s & 7] + (s >> 3) * 256);
#pragma unroll
    for (int s = 0; s < 8; ++s) { qo[s] += L_Q - w * 32768; asm volatile("" : "+v"(qo[s])); }
    bar();
    STAMP(2);
    f32x16 S;
#pragma unroll
    for (int i = 0; i < 16; ++i) S[i] = 0.f;
    u32x4 va[4][8];
    {
      u32x4 qb[4][8];
#pragma unroll
      for (int s = 0; s < 8; ++s) qb[0][s] = LR16(qo[s]);
#pragma unroll
      for (int k = 0; k < 4; ++k) {
        if (k < 3) {
#pragma unroll
          for (int s = 0; s < 8; ++s) qb[k + 1][s] = LR16(qo[s] + (k + 1) * 256);
        }
        sb0();
#pragma unroll
        for (int s = 0; s < 8; ++s) S = mfma32(ca[8 * k + s], qb[k][s], S);
        sb0();
#pragma unroll
        for (int ks = 0; ks < 8; ++ks)
#pragma unroll
          for (int tt = 0; tt < 2; ++tt) {
            const u32x2 v = TR(to[k][tt][ks >> 2] + (ks & 3) * 16384);
            va[k][ks][2 * tt] = v[0]; va[k][ks][2 * tt + 1] = v[1];
          }
      }
      sb0();
    }
    float mx = S[0];
#pragma unroll
    for (int i = 1; i < 16; ++i) mx = fmaxf(mx, S[i]);
    bar();
    *(LAS float*)(L + L_MX + r32 * 32 + (w * 2 + hf) * 4) = mx;
    bar();
    STAMP(13);
    {
      const f32x4 a = *(LAS f32x4*)(L + L_MX + r32 * 32), b = *(LAS f32x4*)(L + L_MX + r32 * 32 + 16);
      mx = fmaxf(fmaxf(fmaxf(a[0], a[1]), fmaxf(a[2], a[3])), fmaxf(fmaxf(b[0], b[1]), fmaxf(b[2], b[3])));
    }
#pragma unroll
    for (int s = 0; s < 2; ++s) {
      u32x4 p;
#pragma unroll
      for (int e = 0; e < 4; ++e) p[e] = pk(__builtin_amdgcn_exp2f(S[8 * s + 2 * e] - mx), __builtin_amdgcn_exp2f(S[8 * s + 2 * e + 1] - mx));
      LR16(L_P + (w * 2 + s) * 1024 + lane * 16) = p;
    }
    unsigned sw[4], sr = 65536 + w * 8192 + (lane >> 3) * 128 + (((lane ^ (lane >> 3)) & 7) << 4);
    unsigned so = WS_PART + (unsigned)((lane >> 3) * 8 + 2 * w) * PST + (unsigned)((ab * 64 + aj) * 128 + (lane & 7) * 16);
#pragma unroll
    for (int i = 0; i < 4; ++i) { sw[i] = 65536 + w * 8192 + r32 * 128 + ((((i >> 1) * 4 + hf * 2 + (i & 1)) ^ (r32 & 7)) << 4); asm volatile("" : "+v"(sw[i])); }
    asm volatile("" : "+v"(sr), "+v"(so));
    bar();
    STAMP(8);
    float lsum;
    {
      u32x4 pf[8];
#pragma unroll
      for (int ks = 0; ks < 8; ++ks) pf[ks] = LR16(L_P + ks * 1024 + lane * 16);
      f32x16 O[5];
#pragma unroll
      for (int t = 0; t < 5; ++t)
#pragma unroll
        for (int i = 0; i < 16; ++i) O[t][i] = 0.f;
#pragma unroll
      for (int ks = 0; ks < 8; ++ks) { O[0] = mfma32(va[0][ks], pf[ks], O[0]); O[1] = mfma32(va[1][ks], pf[ks], O[1]); }
      sb0();
      const u32x4 ones = {0x3f803f80u, 0x3f803f80u, 0x3f803f80u, 0x3f803f80u};
#pragma unroll
      for (int pr = 0; pr < 2; ++pr) {
#pragma unroll
        for (int i = 0; i < 4; ++i) {
          const int t = 2 * pr + (i >> 1), u = i & 1;
          if (pr == 0) {
#pragma unroll
            for (int ks = 2 * i; ks < 2 * i + 2; ++ks) { O[2] = mfma32(va[2][ks], pf[ks], O[2]); O[3] = mfma32(va[3][ks], pf[ks], O[3]); }
          }
          LR16(sw[i] + pr * 4096) = u32x4{pk(O[t][8 * u], O[t][8 * u + 1]), pk(O[t][8 * u + 2], O[t][8 * u + 3]), pk(O[t][8 * u + 4], O[t][8 * u + 5]), pk(O[t][8 * u + 6], O[t][8 * u + 7])};
          sb0();
        }
        u32x4 v[4];
#pragma unroll
        for (int i = 0; i < 4; ++i) v[i] = LR16(sr + pr * 4096 + i * 1024);
        sb0();
#pragma unroll
        for (int i = 0; i < 4; ++i) __builtin_amdgcn_raw_buffer_store_b128(v[i], rws, so, (i * 64 + pr) * PST, 16);
        sb0();
      }
#pragma unroll
      for (int ks = 0; ks < 8; ++ks) O[4] = mfma32(ones, pf[ks], O[4]);
      lsum = O[4][0];
    }
    STAMP(3);
#pragma unroll
    for (int e = 0; e < 4; ++e) msk[e] = ((n16 & 7) >> 1) == e ? ((n16 & 1) ? 0xffff0000u : 0xffffu) : 0u;
    drain();
    bar();
    if (w == 0 && lane < 32) { u32x2 v = {canon(mx), canon(lsum)}; st8c(rws, WS_ML + (unsigned)((ab * 64 + aj) * 32 + r32) * 8, v); }
    STAMP(4);
    u32x2 ml = {SENT, SENT};
    for (unsigned it = 0; it < SPIN_CAP; ++it) {
      __builtin_amdgcn_s_sleep(ML_SLEEP);
      fence_c();
      if (ml[0] == SENT || ml[1] == SENT) ml = ld8c(rws, WS_ML + (unsigned)((w * 64 + lane) * 32 + h) * 8);
      if (!__any(ml[0] == SENT || ml[1] == SENT)) break;
    }
    fence_c();
    STAMP(5);
    u32x4 pv[8];
#pragma unroll
    for (int i = 0; i < 8; ++i)
      pv[i] = ld16c(rws, WS_PART + (unsigned)(h * 8 + sl) * PST + (unsigned)((w * 64 + 8 * i) * 128 + lane * 16));
    fence_c();
    {
      const float m = __uint_as_float(ml[0]);
      float M = m;
      M = fmaxf(M, dppf<0x128>(M)); M = fmaxf(M, dppf<0x124>(M)); M = fmaxf(M, dppf<0x122>(M)); M = fmaxf(M, dppf<0x121>(M));
      M = max16(M); M = max32(M);
      const float we = __builtin_amdgcn_exp2f(m - M);
      const unsigned wp = pk(we, we);
      float l = bfhi(wp) * __uint_as_float(ml[1]);
      l += dppf<0x128>(l); l += dppf<0x124>(l); l += dppf<0x122>(l); l += dppf<0x121>(l);
      l = sum16(l); l = sum32(l);
      const float inv = 1.f / l;
      u32x4 wb[8];
#pragma unroll
      for (int i = 0; i < 8; ++i) {
        wb[i] = pick(msk, (unsigned)__builtin_amdgcn_ds_bpermute((8 * i + (lane >> 3)) * 4, (int)wp));
        asm volatile("" : "+v"(wb[i]));
      }
      sb0();
      f32x4 d[2] = {{0.f, 0.f, 0.f, 0.f}, {0.f, 0.f, 0.f, 0.f}};
#pragma unroll
      for (int i = 0; i < 8; ++i) d[i & 1] = mfma16(wb[i], pv[i], d[i & 1]);
      const f32x4 c = fold(d[0] + d[1]);
      if (g < 2 && n16 < 8) *(LAS u32x2*)(L + L_CC + w * 128 + n16 * 16 + g * 8) = u32x2{pk(c[0] * inv, c[1] * inv), pk(c[2] * inv, c[3] * inv)};
    }
    STAMP(6);
    bar();
    if (blk >= 224) {
      const int lc = blk - 224;
      const u32x4 xr = *(const u32x4*)(x + lc * 64 + (lane & 7) * 8);
      LAS f32x4* rs = (LAS f32x4*)(L + L_RS + w * 256 + n16 * 32 + g * 16);
      f32x4 acc = line_sum<8>(rws, WS_P2 + lc * PST, w * 64, 256, lane, msk);
      STAMP(7);
      if (g < 2 && n16 < 8) rs[0] = acc;
      bar();
      u32x4 hb;
      f32x4 t[8];
      if (w == 0) {
        f32x4 lo = {bflo(xr[0]), bfhi(xr[0]), bflo(xr[1]), bfhi(xr[1])}, hi = {bflo(xr[2]), bfhi(xr[2]), bflo(xr[3]), bfhi(xr[3])};
#pragma unroll
        for (int k = 0; k < 8; ++k) t[k] = *(LAS f32x4*)(L + L_RS + (k >> 1) * 256 + (lane & 7) * 32 + (k & 1) * 16);
        sb0();
        lo += (t[0] + t[2]) + (t[4] + t[6]); hi += (t[1] + t[3]) + (t[5] + t[7]);
        hb = u32x4{pk(lo[0], lo[1]), pk(lo[2], lo[3]), pk(hi[0], hi[1]), pk(hi[2], hi[3])};
        st16c(rws, WS_HB + (unsigned)((lane >> 3) * 4096 + lc * 128 + (lane & 7) * 16), u32x4{canon2(hb[0]), canon2(hb[1]), canon2(hb[2]), canon2(hb[3])});
      }
      STAMP(8);
      __builtin_amdgcn_s_sleep(24);
      acc = line_sum<3>(rws, WS_P4 + lc * (NFFN * 128), w * 24, NFFN, lane, msk);
      STAMP(11);
      if (g < 2 && n16 < 8) rs[64] = acc;
      bar();
      if (w == 0 && lane < 8) {
        f32x4 lo = {bflo(hb[0]), bfhi(hb[0]), bflo(hb[1]), bfhi(hb[1])}, hi = {bflo(hb[2]), bfhi(hb[2]), bflo(hb[3]), bfhi(hb[3])};
#pragma unroll
        for (int k = 0; k < 8; ++k) t[k] = *(LAS f32x4*)(L + L_RS + 1024 + (k >> 1) * 256 + lane * 32 + (k & 1) * 16);
        sb0();
        lo += (t[0] + t[2]) + (t[4] + t[6]); hi += (t[1] + t[3]) + (t[5] + t[7]);
        *(u32x4*)(out + lc * 64 + lane * 8) = u32x4{pk(lo[0], lo[1]), pk(lo[2], lo[3]), pk(hi[0], hi[1]), pk(hi[2], hi[3])};
      }
      STAMP(12);
    }
    STAMP_FLUSH;
  } else {
    const int a = wave - 4;
    volatile LAS unsigned* syn = (volatile LAS unsigned*)(L + L_SY + a * 1024);
    const unsigned tag = __builtin_amdgcn_raw_buffer_load_b32(rws, WS_EPOCH, 0, 16) + 1u;
    if (lane < 8) syn[lane] = 0u;
    f32x4 D[8];
    {
      u32x4 wr[4][8];
      const rsrc_t rq = mk(wq, 512 * 4096 * 2);
      const unsigned qrow = (unsigned)((128 * a + 8 * g) * 4096 + h * 128 + n16 * 8) * 2;
#pragma unroll
      for (int s = 0; s < 4; ++s)
#pragma unroll
        for (int e = 0; e < 8; ++e) {
          wr[s][e] = __builtin_amdgcn_raw_buffer_load_b128(rq, qrow, (32 * (s ^ (sl >> 1)) + (e ^ ((sl & 1) * 4))) * 8192, WPOL);
          if (s == 0 && e == 3) {
            fence_c();
#pragma unroll
            for (int i = 0; i < 4; ++i) {
              const int row = 16 * a + 4 * i + g;
              const unsigned key = (row & 3) | (((row >> 3) & 3) << 2);
              dma16<true>(wuk, (unsigned)((64 * sl + row) * 4096 + h * 128 + ((n16 ^ key) << 3)) * 2, lbase + L_WK + (4 * a + i) * 1024);
            }
          }
          if (8 * s + e == WQ_B0) bar();
        }
      fence_c();
      u32x4 xa[4];
#pragma unroll
      for (int s = 0; s < 4; ++s) {
        const unsigned o = L_XN + (n16 >> 2) * 1040 + (128 * a + 32 * (s ^ (sl >> 1)) + 8 * g) * 2;
        const u32x2 v0 = *(LAS u32x2*)(L + o + (sl & 1) * 8), v1 = *(LAS u32x2*)(L + o + 8 - (sl & 1) * 8);
        xa[s] = u32x4{v0[0], v0[1], v1[0], v1[1]};
      }
#pragma unroll
      for (int j = 0; j < 8; ++j) D[j] = f32x4{0.f, 0.f, 0.f, 0.f};
#pragma unroll
      for (int s = 0; s < 4; ++s) {
#pragma unroll
        for (int j = 0; j < 8; ++j) D[j] = mfma16(xa[s], colfrag(wr[s], j), D[j]);
        sb0();
      }
    }
    STAMP(1);
    LR16(L_QP + a * 1088 + g * 272 + n16 * 16) = u32x4{pk(D[0][0], D[1][0]), pk(D[2][0], D[3][0]), pk(D[4][0], D[5][0]), pk(D[6][0], D[7][0])};
    asm volatile("s_waitcnt lgkmcnt(0)" ::: "memory");
    if (lane < 2) *(volatile LAS unsigned*)(L + L_SY + lane * 1024 + a * 4) = 1u;
    if (a < 2) {
      unsigned ko[4];
#pragma unroll
      for (int s = 0; s < 4; ++s) { ko[s] = L_WK + (8 * (n16 >> 2) + 4 * a + (n16 & 3)) * 256 + (((4 * s + g) ^ n16) << 4); asm volatile("" : "+v"(ko[s])); }
      for (unsigned it = 0; it < SPIN_CAP * 16; ++it) {
        const u32x4 f = *(volatile LAS u32x4*)syn;
        if (f[0] & f[1] & f[2] & f[3] & syn[6]) break;
      }
      fence_c();
      STAMP(2);
      u32x4 qp[4][4], wk[2][4];
#pragma unroll
      for (int s = 0; s < 4; ++s) {
#pragma unroll
        for (int pr = 0; pr < 2; ++pr) wk[pr][s] = LR16(ko[s] + pr * 8192);
#pragma unroll
        for (int k = 0; k < 4; ++k) qp[s][k] = LR16(L_QP + (lane & 3) * 272 + g * 16 + k * 1088 + s * 64);
      }
      sb0();
      f32x4 d[2][2] = {{{0.f, 0.f, 0.f, 0.f}, {0.f, 0.f, 0.f, 0.f}}, {{0.f, 0.f, 0.f, 0.f}, {0.f, 0.f, 0.f, 0.f}}};
#pragma unroll
      for (int s = 0; s < 4; ++s)
#pragma unroll
        for (int k = 0; k < 4; ++k)
#pragma unroll
          for (int pr = 0; pr < 2; ++pr) d[pr][k & 1] = mfma16(wk[pr][s], qp[s][k], d[pr][k & 1]);
      const float sc = 0.08838834764831845f * 1.4426950408889634f;
      const f32x4 v = ((n16 & 4) ? d[1][0] + d[1][1] : d[0][0] + d[0][1]) * sc;
      if (n16 < 8)
        st16c(rws, WS_QA + (unsigned)((lane & 3) * 65536 + h * 2048 + a * 1024 + sl * 128 + (n16 >> 2) * 64 + g * 16), u32x4{pk(v[0], v[1]), tag, pk(v[2], v[3]), tag});
      if (lane < 4) *(volatile LAS unsigned*)(L + L_SY + lane * 1024 + 16 + a * 4) = 1u;
      STAMP(3);
    }
    for (unsigned it = 0; it < SPIN_CAP * 16; ++it) {
      if (syn[4] & syn[5]) break;
      __builtin_amdgcn_s_sleep(1);
    }
    __builtin_amdgcn_s_sleep(QA_SLEEP);
    {
      unsigned pend = 0xffu;
      const unsigned qoff = WS_QA + (unsigned)(ab * 65536 + a * 2048 + lane * 16);
      for (unsigned it = 0; it < SPIN_CAP; ++it) {
        u32x4 qv[8][2];
        fence_c();
#pragma unroll
        for (int i = 0; i < 8; ++i)
          if (pend & (1u << i)) { qv[i][0] = ld16c(rws, qoff + i * 8192); qv[i][1] = ld16c(rws, qoff + i * 8192 + 1024); }
#pragma unroll
        for (int i = 0; i < 8; ++i)
          if (pend & (1u << i)) {
            LR16(L_Q + (4 * i + a) * 1024 + ((lane ^ ((a << 2) | (i & 3))) << 4)) = u32x4{qv[i][0][0], qv[i][0][2], qv[i][1][0], qv[i][1][2]};
            const unsigned bad = (qv[i][0][1] ^ tag) | (qv[i][0][3] ^ tag) | (qv[i][1][1] ^ tag) | (qv[i][1][3] ^ tag);
            if (!__any(bad != 0u)) pend &= ~(1u << i);
          }
        if (it == 0) STAMP(4);
        if (!pend) break;
        __builtin_amdgcn_s_sleep(4);
      }
    }
    fence_c();
    STAMP(6);
    u32x4 uv[4], wor[8];
    {
      const rsrc_t rv = mk(wuv, 512 * 4096 * 2), ro = mk(wo, 4096 * 512 * 2);
#pragma unroll
      for (int i = 0; i < 4; ++i) uv[i] = ld16n(rv, (unsigned)((64 * sl + 16 * a + 4 * i + g) * 4096 + h * 128 + n16 * 8) * 2);
#pragma unroll
      for (int e = 0; e < 8; ++e) wor[e] = ld16n(ro, (unsigned)((h * 128 + sl * 16 + (g & 1) * 8 + e) * 512 + 128 * a + n16 * 8) * 2);
    }
    fence_c();
    bar();
    if (blk == 0 && a == 0 && lane == 0) __builtin_amdgcn_raw_buffer_store_b32(tag, rws, WS_EPOCH, 0, 16);
    u32x4 gu[8], dn[8], gf[2][4], df[8];
    const rsrc_t rg = mk(wg, 512 * DF * 2), ru = mk(wu, 512 * DF * 2), rd = mk(wd, DF * 512 * 2);
    const unsigned go = (unsigned)((a * 128 + (lane >> 1)) * DF + fi * 16 + (lane & 1) * 8) * 2;
    if (ffn) {
#pragma unroll
      for (int i = 0; i < 4; ++i) gu[i] = ld16(rg, go + i * (32 * DF * 2));
    }
    bar();
    bar();
    if (ffn) { gu[4] = ld16(ru, go); gu[5] = ld16(ru, go + 32 * DF * 2); }
    bar();
    if (ffn) {
      gu[6] = ld16(ru, go + 64 * DF * 2); gu[7] = ld16(ru, go + 96 * DF * 2);
#pragma unroll
      for (int e = 0; e < 8; ++e) dn[e] = ld16(rd, (unsigned)((fi * 16 + (g & 1) * 8 + e) * 512 + a * 128 + n16 * 8) * 2);
    }
    fence_c();
#pragma unroll
    for (int i = 0; i < 4; ++i) LR16(L_UV + (16 * a + 4 * i + g) * 272 + n16 * 16) = uv[i];
    bar();
    u32x4 vf[2][2], of[8];
#pragma unroll
    for (int tt = 0; tt < 2; ++tt)
#pragma unroll
      for (int s = 0; s < 2; ++s) {
        const unsigned ad = L_UV + (32 * s + 8 * g + (n16 >> 2)) * 272 + a * 64 + tt * 32 + (lane & 3) * 8;
        const u32x2 v0 = TR(ad), v1 = TR(ad + 4 * 272);
        vf[tt][s] = u32x4{v0[0], v0[1], v1[0], v1[1]};
        asm volatile("" : "+v"(vf[tt][s]));
      }
#pragma unroll
    for (int j = 0; j < 8; ++j) { of[j] = colfrag(wor, j); asm volatile("" : "+v"(of[j])); }
    unsigned xo = WS_CX + (unsigned)(((h * 8 + 2 * a + ((n16 >> 2) & 1)) * 8 + sl) * 128 + (lane & 3) * 32 + g * 8);
    unsigned xi = WS_CX + (unsigned)(((h * 8 + sl) * 8 + (g >> 1)) * 128 + (n16 >> 2) * 32 + (g & 1) * 16);
    unsigned po = WS_P2 + (unsigned)(g * 8 + 2 * a + (n16 >> 3)) * PST + (unsigned)(blk * 128 + (n16 & 7) * 16);
    asm volatile("" : "+v"(xo), "+v"(xi), "+v"(po));
    LAS float* red = (LAS float*)(L + L_RED + a * 704);
    if (ffn) {
      float csum[2];
      {
        const unsigned sb = L_GU + a * 8192;
#pragma unroll
        for (int i = 0; i < 8; ++i) LR16(sb + i * 1024 + lane * 16) = gu[i];
        const u32x4 ones = {0x3f803f80u, 0x3f803f80u, 0x3f803f80u, 0x3f803f80u};
#pragma unroll
        for (int m = 0; m < 2; ++m) {
          f32x4 c = {0.f, 0.f, 0.f, 0.f};
#pragma unroll
          for (int s = 0; s < 4; ++s) {
            const unsigned ad = sb + m * 4096 + (32 * s + 8 * g + (n16 >> 2)) * 32 + (lane & 3) * 8;
            const u32x2 v0 = TR(ad), v1 = TR(ad + 128);
            gf[m][s] = u32x4{v0[0], v0[1], v1[0], v1[1]};
            c = mfma16(ones, gf[m][s], c);
            asm volatile("" : "+v"(gf[m][s]));
          }
          csum[m] = c[0];
        }
#pragma unroll
        for (int j = 0; j < 8; ++j) { df[j] = colfrag(dn, j); asm volatile("" : "+v"(df[j])); }
      }
      if (g < 2) red[128 + g * 16 + n16] = g ? csum[1] : csum[0];
    }
    STAMP(5);
    bar();
    {
      const u32x4 c0 = LR16(L_CC + (lane & 3) * 128 + g * 16), c1 = LR16(L_CC + (lane & 3) * 128 + 64 + g * 16);
      const f32x4 d0 = mfma16(vf[0][1], c1, mfma16(vf[0][0], c0, f32x4{0.f, 0.f, 0.f, 0.f}));
      const f32x4 d1 = mfma16(vf[1][1], c1, mfma16(vf[1][0], c0, f32x4{0.f, 0.f, 0.f, 0.f}));
      const f32x4 d = (n16 & 4) ? d1 : d0;
      if (n16 < 8) st8c(rws, xo, u32x2{canon2(pk(d[0], d[1])), canon2(pk(d[2], d[3]))});
      u32x4 cf[4];
      for (unsigned it = 0; it < SPIN_CAP; ++it) {
        __builtin_amdgcn_s_sleep(2);
        fence_c();
        unsigned m = 0;
#pragma unroll
        for (int s = 0; s < 4; ++s) cf[s] = ld16c(rws, xi + s * 256);
#pragma unroll
        for (int s = 0; s < 4; ++s) m = max4u(m, cf[s]);
        if (!__any(m == SENT)) break;
      }
      STAMP(7);
      f32x4 o[8];
#pragma unroll
      for (int j = 0; j < 8; ++j) o[j] = f32x4{0.f, 0.f, 0.f, 0.f};
#pragma unroll
      for (int s = 0; s < 4; ++s)
#pragma unroll
        for (int j = 0; j < 8; ++j) o[j] = mfma16(cf[s], of[j], o[j]);
      st16c(rws, po, u32x4{canon2(pk(o[0][0], o[1][0])), canon2(pk(o[2][0], o[3][0])), canon2(pk(o[4][0], o[5][0])), canon2(pk(o[6][0], o[7][0]))});
    }
    STAMP(8);
    if (ffn) {
      u32x4 hv[4];
      for (unsigned it = 0; it < SPIN_CAP; ++it) {
        fence_c();
        unsigned m = 0;
#pragma unroll
        for (int s = 0; s < 4; ++s) hv[s] = ld16c(rws, WS_HB + (unsigned)((blk & 7) * 4096 + ((n16 >> 2) * 512 + a * 128 + s * 32 + g * 8) * 2));
#pragma unroll
        for (int s = 0; s < 4; ++s) m = max4u(m, hv[s]);
        if (!__any(m == SENT)) break;
        __builtin_amdgcn_s_sleep(2);
      }
      STAMP(9);
      {
        f32x4 G = {0.f, 0.f, 0.f, 0.f}, U = {0.f, 0.f, 0.f, 0.f};
        float s1 = 0.f, s2 = 0.f;
#pragma unroll
        for (int s = 0; s < 4; ++s) {
          G = mfma16(hv[s], gf[0][s], G); U = mfma16(hv[s], gf[1][s], U);
#pragma unroll
          for (int e = 0; e < 4; ++e) { const float lo = bflo(hv[s][e]), hi = bfhi(hv[s][e]); s1 += lo + hi; s2 += lo * lo + hi * hi; }
        }
        s1 = sum32(sum16(s1)); s2 = sum32(sum16(s2));
        red[lane] = G[0];
        red[64 + lane] = U[0];
        if (lane < 16) { red[160 + (lane >> 2) * 2] = s1; red[161 + (lane >> 2) * 2] = s2; }
      }
      bar();
      float G = 0.f, U = 0.f, cg = 0.f, cu = 0.f, s1 = 0.f, s2 = 0.f;
#pragma unroll
      for (int k = 0; k < 4; ++k) {
        LAS float* rk = (LAS float*)(L + L_RED + k * 704);
        G += rk[lane]; U += rk[64 + lane]; cg += rk[128 + n16]; cu += rk[144 + n16];
        s1 += rk[160 + g * 2]; s2 += rk[161 + g * 2];
      }
      const float mean = s1 * (1.f / 512.f);
      const float var = fmaxf(s2 * (1.f / 512.f) - mean * mean, 0.f);
      const float rstd = __builtin_amdgcn_rsqf(var + 1e-5f);
      const float gg = (G - mean * cg) * rstd, uu = (U - mean * cu) * rstd;
      const float z = 0.7978845608028654f * (gg + 0.044715f * gg * gg * gg);
      const float th = 1.f - 2.f * __builtin_amdgcn_rcpf(1.f + __expf(2.f * z));
      const float hh = 0.5f * gg * (1.f + th) * uu;
      *(LAS uint16_t*)(L + L_HS + a * 128 + lane * 2) = (uint16_t)pk(hh, 0.f);
      fence_c();
      u32x4 ha = LR16(L_HS + a * 128 + (n16 >> 2) * 32 + (g & 1) * 16);
      if (g >= 2) ha = u32x4{0u, 0u, 0u, 0u};
      float o[8];
#pragma unroll
      for (int j = 0; j < 8; ++j) o[j] = mfma16(ha, df[j], f32x4{0.f, 0.f, 0.f, 0.f})[0];
      st16c(rws, WS_P4 + (unsigned)(((g * 8 + 2 * a + (n16 >> 3)) * NFFN + fi) * 128 + (n16 & 7) * 16),
            u32x4{canon2(pk(o[0], o[1])), canon2(pk(o[2], o[3])), canon2(pk(o[4], o[5])), canon2(pk(o[6], o[7]))});
      STAMP(10);
    }
    STAMP_FLUSH;
  }
}

extern "C" void kernel_launch(void* const* d_in, const int* in_sizes, int n_in, void* d_out, int out_size, void* d_ws,
                              size_t ws_size, hipStream_t stream) {
  fused_kernel<<<256, 512, 0, stream>>>((const uint16_t*)d_in[0], (const uint16_t*)d_in[1], (const uint16_t*)d_in[2],
                                        (const uint16_t*)d_in[3], (const uint16_t*)d_in[4], (const uint16_t*)d_in[5],
                                        (const uint16_t*)d_in[7], (const uint16_t*)d_in[8], (const uint16_t*)d_in[9],
                                        (uint16_t*)d_out, (char*)d_ws);
}
```

```cpp
#include <hip/hip_runtime.h>
#include <hip/hip_bf16.h>
#include <stdint.h>

typedef __bf16 bf16x8 __attribute__((ext_vector_type(8)));
typedef __bf16 bf16x2 __attribute__((ext_vector_type(2)));
typedef short s16x4 __attribute__((ext_vector_type(4)));
typedef float f32x2 __attribute__((ext_vector_type(2)));
typedef float f32x4 __attribute__((ext_vector_type(4)));
typedef float f32x16 __attribute__((ext_vector_type(16)));
typedef unsigned u32x2 __attribute__((ext_vector_type(2)));
typedef unsigned u32x4 __attribute__((ext_vector_type(4)));
typedef __amdgpu_buffer_rsrc_t rsrc_t;
#define DI __device__ __forceinline__
#define LAS __attribute__((address_space(3)))

#define CKV_WIN 5
#define WPOL 0
#define TPOL ""
#define QA_SLEEP 32
#define QX_PRE 8
#define QX_SLEEP 4
#define CX_SLEEP 4
#define ML_SLEEP 2

constexpr int DF = 1376;
constexpr int NFFN = 86;
constexpr unsigned PST = 32768 + 256;
constexpr unsigned WS_EPOCH = 0;
constexpr unsigned WS_QA = 4096;
constexpr unsigned WS_QX = WS_QA + 262144;
constexpr unsigned WS_CLR = WS_QX + 524288;
constexpr unsigned WS_ML = WS_CLR;
constexpr unsigned WS_HB = WS_ML + 65536;
constexpr unsigned WS_CX = WS_HB + 32768;
constexpr unsigned WS_P4 = WS_CX + 262144;
constexpr unsigned WS_P2 = WS_P4 + 32 * NFFN * 128;
constexpr unsigned CLR_PER_WG = 7168;
static_assert(WS_P2 + 32 * PST <= WS_CLR + 256 * CLR_PER_WG, "clear span");
constexpr unsigned WS_PART = WS_CLR + 256 * CLR_PER_WG;
constexpr unsigned WS_DBG = WS_PART + 256 * PST;
constexpr unsigned WS_END = WS_DBG + 256 * 8 * 16 * 8;
constexpr unsigned SENT = 0xFFFFFFFFu;
constexpr unsigned SPIN_CAP = 1u << 18;

constexpr unsigned L_Q = 131072;
constexpr unsigned L_WK = L_Q;
constexpr unsigned L_QP = L_Q + 16384;
constexpr unsigned L_SY = L_Q + 28 * 1024;
constexpr unsigned L_P = L_Q;
constexpr unsigned L_MX = L_Q + 8192;
constexpr unsigned L_LS = L_Q + 9216;
constexpr unsigned L_OS = L_Q + 12288;
constexpr unsigned L_CC = L_Q + 28672;
constexpr unsigned L_RED = L_Q + 8192;
constexpr unsigned L_HS = L_Q + 30720;
constexpr unsigned L_UV = 0;
constexpr unsigned L_GU = 32768;
constexpr unsigned L_RS = 65536;

#define STAMP(i) do {} while (0)
#define STAMP_DECL
#define STAMP_FLUSH

DI rsrc_t mk(const void* p, unsigned bytes) { return __builtin_amdgcn_make_buffer_rsrc((void*)p, 0, bytes, 0x00020000); }
DI u32x4 ld16(rsrc_t r, unsigned off) { return __builtin_amdgcn_raw_buffer_load_b128(r, off, 0, WPOL); }
DI u32x4 ld16n(rsrc_t r, unsigned off) { return __builtin_amdgcn_raw_buffer_load_b128(r, off, 0, 2); }
DI u32x4 ld16c(rsrc_t r, unsigned off) { return __builtin_amdgcn_raw_buffer_load_b128(r, off, 0, 16); }
DI u32x2 ld8c(rsrc_t r, unsigned off) { return __builtin_amdgcn_raw_buffer_load_b64(r, off, 0, 16); }
DI void st16c(rsrc_t r, unsigned off, u32x4 v) { __builtin_amdgcn_raw_buffer_store_b128(v, r, off, 0, 16); }
DI void st8c(rsrc_t r, unsigned off, u32x2 v) { __builtin_amdgcn_raw_buffer_store_b64(v, r, off, 0, 16); }
DI void fence_c() { asm volatile("" ::: "memory"); }
DI void bar() { asm volatile("s_waitcnt lgkmcnt(0)\n\ts_barrier" ::: "memory"); }
DI void drain() { asm volatile("s_waitcnt vmcnt(0)" ::: "memory"); }
template <int N> DI void wait_vm() { asm volatile("s_waitcnt vmcnt(%0)" ::"n"(N) : "memory"); }
template <bool NT> DI void dma16(const void* sbase, unsigned voff, unsigned lds_dst) {
  unsigned keep;
  if (NT)
    asm volatile("s_mov_b32 %0, m0\n\ts_mov_b32 m0, %3\n\ts_nop 0\n\tglobal_load_lds_dwordx4 %1, %2 nt\n\ts_mov_b32 m0, %0"
                 : "=&s"(keep) : "v"(voff), "s"(sbase), "s"(lds_dst) : "memory");
  else
    asm volatile("s_mov_b32 %0, m0\n\ts_mov_b32 m0, %3\n\ts_nop 0\n\tglobal_load_lds_dwordx4 %1, %2\n\ts_mov_b32 m0, %0"
                 : "=&s"(keep) : "v"(voff), "s"(sbase), "s"(lds_dst) : "memory");
}
DI float bflo(unsigned u) { return __uint_as_float(u << 16); }
DI float bfhi(unsigned u) { return __uint_as_float(u & 0xffff0000u); }
DI unsigned pk(float a, float b) { f32x2 f = {a, b}; return __builtin_bit_cast(unsigned, __builtin_convertvector(f, bf16x2)); }
DI unsigned canon(float v) { unsigned u = __float_as_uint(v); return u == SENT ? 0x7fc00000u : u; }
DI unsigned canon2(unsigned u) { return u == SENT ? 0x7fc07fc0u : u; }
DI float dot2(unsigned a, unsigned b, float c) { return __builtin_amdgcn_fdot2_f32_bf16(__builtin_bit_cast(bf16x2, a), __builtin_bit_cast(bf16x2, b), c, false); }
DI void sb0() { __builtin_amdgcn_sched_barrier(0); }
template <int C> DI float dppf(float x) { return __int_as_float(__builtin_amdgcn_update_dpp(0, __float_as_int(x), C, 0xf, 0xf, true)); }
DI float sum16(float x) { auto r = __builtin_amdgcn_permlane16_swap(__float_as_uint(x), __float_as_uint(x), false, false); return __uint_as_float(r[0]) + __uint_as_float(r[1]); }
DI float sum32(float x) { auto r = __builtin_amdgcn_permlane32_swap(__float_as_uint(x), __float_as_uint(x), false, false); return __uint_as_float(r[0]) + __uint_as_float(r[1]); }
DI float max16(float x) { auto r = __builtin_amdgcn_permlane16_swap(__float_as_uint(x), __float_as_uint(x), false, false); return fmaxf(__uint_as_float(r[0]), __uint_as_float(r[1])); }
DI float max32(float x) { auto r = __builtin_amdgcn_permlane32_swap(__float_as_uint(x), __float_as_uint(x), false, false); return fmaxf(__uint_as_float(r[0]), __uint_as_float(r[1])); }
DI f32x4 mfma16(u32x4 a, u32x4 b, f32x4 c) { return __builtin_amdgcn_mfma_f32_16x16x32_bf16(__builtin_bit_cast(bf16x8, a), __builtin_bit_cast(bf16x8, b), c, 0, 0, 0); }
DI f32x16 mfma32(u32x4 a, u32x4 b, f32x16 c) { return __builtin_amdgcn_mfma_f32_32x32x16_bf16(__builtin_bit_cast(bf16x8, a), __builtin_bit_cast(bf16x8, b), c, 0, 0, 0); }
DI float sel4(f32x4 v, int g) { float a = (g & 1) ? v[1] : v[0], b = (g & 1) ? v[3] : v[2]; return (g & 2) ? b : a; }
DI bool bad4(u32x4 v) { return v[0] == SENT || v[1] == SENT || v[2] == SENT || v[3] == SENT; }
DI u32x4 colfrag(const u32x4* r, int j) {
  const unsigned sel = (j & 1) ? 0x07060302u : 0x05040100u;
  u32x4 o;
#pragma unroll
  for (int e = 0; e < 4; ++e) o[e] = __builtin_amdgcn_perm(r[2 * e + 1][j >> 1], r[2 * e][j >> 1], sel);
  return o;
}

#define AUXF 3
#define ATTF 1
#define TOUCH 0
#define MASKED 0
DI unsigned max4u(unsigned m, u32x4 v) { return max(max(max(m, v[0]), v[1]), max(v[2], v[3])); }
DI u32x4 pick(const u32x4 msk, unsigned ww) { return u32x4{msk[0] & ww, msk[1] & ww, msk[2] & ww, msk[3] & ww}; }
DI f32x4 fold(f32x4 d) {
#pragma unroll
  for (int r = 0; r < 4; ++r) {
    const unsigned t = __float_as_uint(dppf<0x128>(d[r]));
    d[r] += __uint_as_float(__builtin_amdgcn_permlane32_swap(t, t, false, false)[1]);
  }
  return d;
}
template <int N, int SL> DI void poll16(rsrc_t rws, const unsigned* off, u32x4* v) {
  bool bad[N];
#pragma unroll
  for (int i = 0; i < N; ++i) { v[i] = u32x4{SENT, SENT, SENT, SENT}; bad[i] = true; }
  for (unsigned it = 0; it < SPIN_CAP; ++it) {
    fence_c();
#pragma unroll
    for (int i = 0; i < N; ++i)
      if (MASKED ? bad[i] : true) v[i] = ld16c(rws, off[i]);
    bool any = false;
#pragma unroll
    for (int i = 0; i < N; ++i) { bad[i] = max4u(0u, v[i]) == SENT; any |= bad[i]; }
    if (!__any(any)) break;
    if (SL) __builtin_amdgcn_s_sleep(SL);
  }
  fence_c();
}
DI void pv_store(LAS char* L, rsrc_t rws, const u32x4 (*va)[8], u32x4* pf, const unsigned* sw, unsigned sr, unsigned so, int lane) {
#pragma unroll
  for (int ks = 0; ks < 8; ++ks) pf[ks] = *(LAS u32x4*)(L + L_P + ks * 1024 + lane * 16);
  f32x16 O[2];
#pragma unroll
  for (int t = 0; t < 2; ++t) {
#pragma unroll
    for (int i = 0; i < 16; ++i) O[t][i] = 0.f;
#pragma unroll
    for (int ks = 0; ks < 8; ++ks) O[t] = mfma32(va[t][ks], pf[ks], O[t]);
  }
#pragma unroll
  for (int i = 0; i < 4; ++i) {
    const int t = i >> 1, u = i & 1;
    *(LAS u32x4*)(L + sw[i]) = u32x4{pk(O[t][8 * u], O[t][8 * u + 1]), pk(O[t][8 * u + 2], O[t][8 * u + 3]), pk(O[t][8 * u + 4], O[t][8 * u + 5]), pk(O[t][8 * u + 6], O[t][8 * u + 7])};
  }
  u32x4 v[4];
#pragma unroll
  for (int i = 0; i < 4; ++i) v[i] = *(LAS u32x4*)(L + sr + i * 1024);
  sb0();
#pragma unroll
  for (int i = 0; i < 4; ++i) __builtin_amdgcn_raw_buffer_store_b128(v[i], rws, so, i * 64 * PST, 16);
  sb0();
}
template <int N> DI f32x4 line_sum(rsrc_t rws, unsigned base, int first, int count, int lane, const u32x4 msk) {
  u32x4 v[N], one[N];
  unsigned off[N];
#pragma unroll
  for (int i = 0; i < N; ++i) {
    const int p = first + 8 * i + (lane >> 3);
    off[i] = base + (unsigned)((p < count ? p : 0) * 128 + (lane & 7) * 16);
    one[i] = pick(msk, p < count ? 0x3f803f80u : 0u);
  }
  poll16<N, 0>(rws, off, v);
  f32x4 d[2] = {{0.f, 0.f, 0.f, 0.f}, {0.f, 0.f, 0.f, 0.f}};
#pragma unroll
  for (int i = 0; i < N; ++i) d[i & 1] = mfma16(one[i], v[i], d[i & 1]);
  return fold(d[0] + d[1]);
}

__global__ void __launch_bounds__(512, 2)
fused_kernel(const uint16_t* __restrict__ x, const uint16_t* __restrict__ ckv, const uint16_t* __restrict__ wq,
             const uint16_t* __restrict__ wuk, const uint16_t* __restrict__ wuv, const uint16_t* __restrict__ wo,
             const uint16_t* __restrict__ wg, const uint16_t* __restrict__ wu, const uint16_t* __restrict__ wd,
             uint16_t* __restrict__ out, char* __restrict__ ws) {
  __shared__ __attribute__((aligned(16))) char lds[163840];
  LAS char* L = (LAS char*)lds;
  const unsigned lbase = (unsigned)(size_t)L;
  const int blk = blockIdx.x;
  const int lane = threadIdx.x & 63;
  const int wave = __builtin_amdgcn_readfirstlane(threadIdx.x >> 6);
  const int g = lane >> 4, n16 = lane & 15;
  const rsrc_t rws = mk(ws, WS_END);
  const int h = (blk & 7) * 4 + ((blk >> 3) & 3), sl = blk >> 5;
  const int ab = (blk & 7) >> 1, aj = (blk & 1) * 32 + (blk >> 3);
  const int fi = (blk & 7) * 11 + (blk >> 3);
  const bool ffn = (blk >> 3) < 11 && fi < NFFN;
#define LR16(off) (*(LAS u32x4*)(L + (off)))
#define TR(off) __builtin_bit_cast(u32x2, __builtin_amdgcn_ds_read_tr16_b64_v4i16((LAS s16x4*)(L + (off))))
#define PV_ADDR(wv, pr) \
    unsigned to[2][2][2], sw[4], sr = 65536 + ((wv) * 2 + (pr)) * 4096 + (lane >> 3) * 128 + (((lane ^ (lane >> 3)) & 7) << 4); \
    unsigned so = WS_PART + (unsigned)((lane >> 3) * 8 + 2 * (wv) + (pr)) * PST + (unsigned)((ab * 64 + aj) * 128 + (lane & 7) * 16); \
    _Pragma("unroll") for (int i = 0; i < 8; ++i) { \
      const int k = i >> 2, tt = (i >> 1) & 1, u = i & 1, q = n16 >> 2, p = lane & 3; \
      to[k][tt][u] = (64 * u + 8 * tt + 4 * hf + q) * 1024 + (wv) * 256 + (((8 * (pr) + 4 * k + 2 * (p & 1) + (g & 1)) ^ ((q << 2) | ((2 * tt + hf) & 3))) << 4) + (p >> 1) * 8; \
      asm volatile("" : "+v"(to[k][tt][u])); } \
    _Pragma("unroll") for (int i = 0; i < 4; ++i) { \
      sw[i] = 65536 + ((wv) * 2 + (pr)) * 4096 + r32 * 128 + ((((i >> 1) * 4 + hf * 2 + (i & 1)) ^ (r32 & 7)) << 4); asm volatile("" : "+v"(sw[i])); } \
    asm volatile("" : "+v"(sr), "+v"(so));
#define PV_FRAGS(k) \
    _Pragma("unroll") for (int ks = 0; ks < 8; ++ks) _Pragma("unroll") for (int tt = 0; tt < 2; ++tt) { \
      const u32x2 v_ = TR(to[k][tt][ks >> 2] + (ks & 3) * 16384); va[k][ks][2 * tt] = v_[0]; va[k][ks][2 * tt + 1] = v_[1]; }
  const int r32 = lane & 31, hf = lane >> 5;
  STAMP_DECL
  STAMP(0);

  if (wave < 4) {
    const int w = wave;
    unsigned touch = 0;
    bar();
    STAMP(9);
    if (w == 0) {
#pragma unroll
      for (int i = 0; i < 7; ++i) st16c(rws, WS_CLR + blk * CLR_PER_WG + i * 1024 + lane * 16, u32x4{SENT, SENT, SENT, SENT});
    }
    {
      const char* src = (const char*)ckv + ((size_t)(ab * 8192 + aj * 128 + w * 32) << 10);
#pragma unroll
      for (int r = 0; r < 32; ++r) {
        if (r == CKV_WIN && w == 0) { wait_vm<CKV_WIN>(); if (lane < 2) *(volatile LAS unsigned*)(L + L_SY + lane * 1024 + 24) = 1u; }
        if (r >= CKV_WIN) wait_vm<CKV_WIN - 1>();
        const unsigned f = ((r & 3) << 2) | ((r >> 2) & 3);
        dma16<true>(src, r * 1024 + ((lane ^ f) << 4), lbase + (w * 32 + r) * 1024);
      }
    }
    unsigned qo[8];
    u32x4 msk;
#pragma unroll
    for (int s = 0; s < 8; ++s) qo[s] = (w * 32 + r32) * 1024 + (((2 * s + hf) ^ (((r32 & 3) << 2) | ((r32 >> 2) & 3))) << 4);
    PV_ADDR(w, 0)
    drain();
    asm volatile("" : "+v"(touch));
    STAMP(1);
    u32x4 ca[32];
#pragma unroll
    for (int s = 0; s < 32; ++s) ca[s] = LR16(qo[s & 7] + (s >> 3) * 256);
#pragma unroll
    for (int s = 0; s < 8; ++s) { qo[s] += L_Q - w * 32768; asm volatile("" : "+v"(qo[s])); }
    bar();
    STAMP(2);
    f32x16 S;
#pragma unroll
    for (int i = 0; i < 16; ++i) S[i] = 0.f;
    u32x4 va[2][8];
    {
      u32x4 qb[4][8];
#pragma unroll
      for (int s = 0; s < 8; ++s) qb[0][s] = LR16(qo[s]);
#pragma unroll
      for (int k = 0; k < 4; ++k) {
        if (k < 3) {
#pragma unroll
          for (int s = 0; s < 8; ++s) qb[k + 1][s] = LR16(qo[s] + (k + 1) * 256);
        }
        sb0();
#pragma unroll
        for (int s = 0; s < 8; ++s) S = mfma32(ca[8 * k + s], qb[k][s], S);
        sb0();
        if (ATTF == 0 && k == 1) { PV_FRAGS(0) }
        if (ATTF == 0 && k == 2) { PV_FRAGS(1) }
      }
      sb0();
    }
    float mx = S[0];
#pragma unroll
    for (int i = 1; i < 16; ++i) mx = fmaxf(mx, S[i]);
    bar();
    *(LAS float*)(L + L_MX + r32 * 32 + (w * 2 + hf) * 4) = mx;
    bar();
    STAMP(13);
    {
      const f32x4 a = *(LAS f32x4*)(L + L_MX + r32 * 32), b = *(LAS f32x4*)(L + L_MX + r32 * 32 + 16);
      sb0();
      if (ATTF == 1) { PV_FRAGS(0) PV_FRAGS(1) }
      sb0();
      mx = fmaxf(fmaxf(fmaxf(a[0], a[1]), fmaxf(a[2], a[3])), fmaxf(fmaxf(b[0], b[1]), fmaxf(b[2], b[3])));
    }
#pragma unroll
    for (int s = 0; s < 2; ++s) {
      u32x4 p;
#pragma unroll
      for (int e = 0; e < 4; ++e) p[e] = pk(__builtin_amdgcn_exp2f(S[8 * s + 2 * e] - mx), __builtin_amdgcn_exp2f(S[8 * s + 2 * e + 1] - mx));
      LR16(L_P + (w * 2 + s) * 1024 + lane * 16) = p;
    }
    bar();
    STAMP(10);
    float lsum;
    {
      u32x4 pf[8];
      pv_store(L, rws, va, pf, sw, sr, so, lane);
      const u32x4 ones = {0x3f803f80u, 0x3f803f80u, 0x3f803f80u, 0x3f803f80u};
      f32x16 R;
#pragma unroll
      for (int i = 0; i < 16; ++i) R[i] = 0.f;
#pragma unroll
      for (int ks = 0; ks < 8; ++ks) R = mfma32(ones, pf[ks], R);
      lsum = R[0];
    }
    STAMP(3);
#pragma unroll
    for (int e = 0; e < 4; ++e) msk[e] = ((n16 & 7) >> 1) == e ? ((n16 & 1) ? 0xffff0000u : 0xffffu) : 0u;
    drain();
    bar();
    if (w == 0 && lane < 32) { u32x2 v = {canon(mx), canon(lsum)}; st8c(rws, WS_ML + (unsigned)((ab * 64 + aj) * 32 + r32) * 8, v); }
    STAMP(4);
    u32x2 ml = {SENT, SENT};
    for (unsigned it = 0; it < SPIN_CAP; ++it) {
      __builtin_amdgcn_s_sleep(ML_SLEEP);
      fence_c();
      if (ml[0] == SENT || ml[1] == SENT) ml = ld8c(rws, WS_ML + (unsigned)((w * 64 + lane) * 32 + h) * 8);
      if (!__any(ml[0] == SENT || ml[1] == SENT)) break;
    }
    fence_c();
    STAMP(5);
    u32x4 pv[8];
#pragma unroll
    for (int i = 0; i < 8; ++i)
      pv[i] = ld16c(rws, WS_PART + (unsigned)(h * 8 + sl) * PST + (unsigned)((w * 64 + 8 * i) * 128 + lane * 16));
    fence_c();
    {
      const float m = __uint_as_float(ml[0]);
      float M = m;
      M = fmaxf(M, dppf<0x128>(M)); M = fmaxf(M, dppf<0x124>(M)); M = fmaxf(M, dppf<0x122>(M)); M = fmaxf(M, dppf<0x121>(M));
      M = max16(M); M = max32(M);
      const float we = __builtin_amdgcn_exp2f(m - M);
      const unsigned wp = pk(we, we);
      float l = bfhi(wp) * __uint_as_float(ml[1]);
      l += dppf<0x128>(l); l += dppf<0x124>(l); l += dppf<0x122>(l); l += dppf<0x121>(l);
      l = sum16(l); l = sum32(l);
      float inv = 1.f / l;
      asm volatile("" : "+v"(inv));
      u32x4 wb[8];
#pragma unroll
      for (int i = 0; i < 8; ++i) {
        wb[i] = pick(msk, (unsigned)__builtin_amdgcn_ds_bpermute((8 * i + (lane >> 3)) * 4, (int)wp));
        asm volatile("" : "+v"(wb[i]));
      }
      sb0();
      f32x4 d[2] = {{0.f, 0.f, 0.f, 0.f}, {0.f, 0.f, 0.f, 0.f}};
#pragma unroll
      for (int i = 0; i < 8; ++i) d[i & 1] = mfma16(wb[i], pv[i], d[i & 1]);
      const f32x4 c = fold(d[0] + d[1]);
      if (g < 2 && n16 < 8) *(LAS u32x2*)(L + L_CC + w * 128 + n16 * 16 + g * 8) = u32x2{pk(c[0] * inv, c[1] * inv), pk(c[2] * inv, c[3] * inv)};
    }
    STAMP(6);
    bar();
    if (blk >= 224) {
      const int lc = blk - 224;
      const u32x4 xr = *(const u32x4*)(x + lc * 64 + (lane & 7) * 8);
      LAS f32x4* rs = (LAS f32x4*)(L + L_RS + w * 256 + n16 * 32 + g * 16);
      f32x4 acc = line_sum<8>(rws, WS_P2 + lc * PST, w * 64, 256, lane, msk);
      STAMP(7);
      if (g < 2 && n16 < 8) rs[0] = acc;
      bar();
      u32x4 hb;
      f32x4 t[8];
      if (w == 0) {
        f32x4 lo = {bflo(xr[0]), bfhi(xr[0]), bflo(xr[1]), bfhi(xr[1])}, hi = {bflo(xr[2]), bfhi(xr[2]), bflo(xr[3]), bfhi(xr[3])};
#pragma unroll
        for (int k = 0; k < 8; ++k) t[k] = *(LAS f32x4*)(L + L_RS + (k >> 1) * 256 + (lane & 7) * 32 + (k & 1) * 16);
        sb0();
        lo += (t[0] + t[2]) + (t[4] + t[6]); hi += (t[1] + t[3]) + (t[5] + t[7]);
        hb = u32x4{pk(lo[0], lo[1]), pk(lo[2], lo[3]), pk(hi[0], hi[1]), pk(hi[2], hi[3])};
        st16c(rws, WS_HB + (unsigned)((lane >> 3) * 4096 + lc * 128 + (lane & 7) * 16), u32x4{canon2(hb[0]), canon2(hb[1]), canon2(hb[2]), canon2(hb[3])});
      }
      STAMP(8);
      __builtin_amdgcn_s_sleep(24);
      acc = line_sum<3>(rws, WS_P4 + lc * (NFFN * 128), w * 24, NFFN, lane, msk);
      STAMP(11);
      if (g < 2 && n16 < 8) rs[64] = acc;
      bar();
      if (w == 0 && lane < 8) {
        f32x4 lo = {bflo(hb[0]), bfhi(hb[0]), bflo(hb[1]), bfhi(hb[1])}, hi = {bflo(hb[2]), bfhi(hb[2]), bflo(hb[3]), bfhi(hb[3])};
#pragma unroll
        for (int k = 0; k < 8; ++k) t[k] = *(LAS f32x4*)(L + L_RS + 1024 + (k >> 1) * 256 + lane * 32 + (k & 1) * 16);
        sb0();
        lo += (t[0] + t[2]) + (t[4] + t[6]); hi += (t[1] + t[3]) + (t[5] + t[7]);
        *(u32x4*)(out + lc * 64 + lane * 8) = u32x4{pk(lo[0], lo[1]), pk(lo[2], lo[3]), pk(hi[0], hi[1]), pk(hi[2], hi[3])};
      }
      STAMP(12);
    }
    STAMP_FLUSH;
  } else {
    const int a = wave - 4;
    volatile LAS unsigned* syn = (volatile LAS unsigned*)(L + L_SY + a * 1024);
    const unsigned tag = __builtin_amdgcn_raw_buffer_load_b32(rws, WS_EPOCH, 0, 16) + 1u;
    if (lane < 8) syn[lane] = 0u;
    if (a == 0) {
      u32x4 xs[4], xa[2], wr[2][8];
      const rsrc_t rx = mk(x, 4096), rq = mk(wq, 512 * 4096 * 2);
#pragma unroll
      for (int i = 0; i < 4; ++i) xs[i] = __builtin_amdgcn_raw_buffer_load_b128(rx, (n16 >> 2) * 1024 + (g * 4 + (n16 & 3)) * 64 + i * 16, 0, 0);
#pragma unroll
      for (int s = 0; s < 2; ++s) xa[s] = __builtin_amdgcn_raw_buffer_load_b128(rx, (n16 >> 2) * 1024 + (64 * sl + 32 * s + 8 * g) * 2, 0, 0);
      const unsigned qrow = (unsigned)((64 * sl + 8 * g) * 4096 + h * 128 + n16 * 8) * 2;
#pragma unroll
      for (int s = 0; s < 2; ++s)
#pragma unroll
        for (int e = 0; e < 8; ++e) wr[s][e] = __builtin_amdgcn_raw_buffer_load_b128(rq, qrow, (32 * s + e) * 8192, 2);
      bar();
      float s1 = 0.f, s2 = 0.f;
      const unsigned one2 = 0x3f803f80u;
#pragma unroll
      for (int i = 0; i < 4; ++i)
#pragma unroll
        for (int e = 0; e < 4; ++e) {
          s1 = dot2(xs[i][e], one2, s1); s2 = dot2(xs[i][e], xs[i][e], s2);
        }
      s1 += dppf<0xb1>(s1); s2 += dppf<0xb1>(s2); s1 += dppf<0x4e>(s1); s2 += dppf<0x4e>(s2);
      s1 = sum32(sum16(s1)); s2 = sum32(sum16(s2));
      const float mean = s1 * (1.f / 512.f);
      const float rstd = __builtin_amdgcn_rsqf(fmaxf(s2 * (1.f / 512.f) - mean * mean, 0.f) + 1e-5f);
      f32x4 D[8];
#pragma unroll
      for (int j = 0; j < 8; ++j) D[j] = f32x4{0.f, 0.f, 0.f, 0.f};
#pragma unroll
      for (int s = 0; s < 2; ++s) {
#pragma unroll
        for (int e = 0; e < 4; ++e) xa[s][e] = pk((bflo(xa[s][e]) - mean) * rstd, (bfhi(xa[s][e]) - mean) * rstd);
#pragma unroll
        for (int j = 0; j < 8; ++j) D[j] = mfma16(xa[s], colfrag(wr[s], j), D[j]);
      }
      const unsigned o = WS_QX + (unsigned)((h * 8 + sl) * 2048 + lane * 16);
      st16c(rws, o, u32x4{pk(D[0][0], D[1][0]), tag, pk(D[2][0], D[3][0]), tag});
      st16c(rws, o + 1024, u32x4{pk(D[4][0], D[5][0]), tag, pk(D[6][0], D[7][0]), tag});
      if (lane < 4) *(volatile LAS unsigned*)(L + L_SY + lane * 1024 + 28) = 1u;
    } else {
      if (a < 3) {
#pragma unroll
        for (int i = 0; i < 8; ++i) {
          const int row = 32 * (a - 1) + 4 * i + g;
          const unsigned key = (row & 3) | (((row >> 3) & 3) << 2);
          dma16<true>(wuk, (unsigned)((64 * sl + row) * 4096 + h * 128 + ((n16 ^ key) << 3)) * 2, lbase + L_WK + (8 * (a - 1) + i) * 1024);
        }
      }
      bar();
      for (unsigned it = 0; it < SPIN_CAP * 16; ++it) {
        if (syn[7]) break;
        __builtin_amdgcn_s_sleep(1);
      }
    }
    STAMP(11);
    __builtin_amdgcn_s_sleep(QX_PRE);
    {
      u32x4 t[4];
      for (unsigned it = 0; it < SPIN_CAP; ++it) {
        __builtin_amdgcn_s_sleep(QX_SLEEP);
        fence_c();
#pragma unroll
        for (int i = 0; i < 4; ++i) t[i] = ld16c(rws, WS_QX + (unsigned)((h * 8 + 2 * a) * 2048 + i * 1024 + lane * 16));
        unsigned bad = 0;
#pragma unroll
        for (int i = 0; i < 4; ++i) bad |= (t[i][1] ^ tag) | (t[i][3] ^ tag);
        if (!__any(bad != 0u)) break;
      }
      STAMP(1);
      u32x4 v;
#pragma unroll
      for (int e = 0; e < 4; ++e) {
        const unsigned p = t[e >> 1][2 * (e & 1)], q = t[2 + (e >> 1)][2 * (e & 1)];
        v[e] = pk(bflo(p) + bflo(q), bfhi(p) + bfhi(q));
      }
      LR16(L_QP + a * 1088 + g * 272 + n16 * 16) = v;
    }
    asm volatile("s_waitcnt lgkmcnt(0)" ::: "memory");
    if (lane < 2) *(volatile LAS unsigned*)(L + L_SY + lane * 1024 + a * 4) = 1u;
    if (a < 2) {
      unsigned ko[4];
#pragma unroll
      for (int s = 0; s < 4; ++s) { ko[s] = L_WK + (8 * (n16 >> 2) + 4 * a + (n16 & 3)) * 256 + (((4 * s + g) ^ n16) << 4); asm volatile("" : "+v"(ko[s])); }
      for (unsigned it = 0; it < SPIN_CAP * 16; ++it) {
        const u32x4 f = *(volatile LAS u32x4*)syn;
        if (f[0] & f[1] & f[2] & f[3] & syn[6]) break;
      }
      fence_c();
      STAMP(2);
      u32x4 qp[4][4], wk[2][4];
#pragma unroll
      for (int s = 0; s < 4; ++s) {
#pragma unroll
        for (int pr = 0; pr < 2; ++pr) wk[pr][s] = LR16(ko[s] + pr * 8192);
#pragma unroll
        for (int k = 0; k < 4; ++k) qp[s][k] = LR16(L_QP + (lane & 3) * 272 + g * 16 + k * 1088 + s * 64);
      }
      sb0();
      f32x4 d[2][2] = {{{0.f, 0.f, 0.f, 0.f}, {0.f, 0.f, 0.f, 0.f}}, {{0.f, 0.f, 0.f, 0.f}, {0.f, 0.f, 0.f, 0.f}}};
#pragma unroll
      for (int s = 0; s < 4; ++s)
#pragma unroll
        for (int k = 0; k < 4; ++k)
#pragma unroll
          for (int pr = 0; pr < 2; ++pr) d[pr][k & 1] = mfma16(wk[pr][s], qp[s][k], d[pr][k & 1]);
      const float sc = 0.08838834764831845f * 1.4426950408889634f;
      const f32x4 v = ((n16 & 4) ? d[1][0] + d[1][1] : d[0][0] + d[0][1]) * sc;
      if (n16 < 8)
        st16c(rws, WS_QA + (unsigned)((lane & 3) * 65536 + h * 2048 + a * 1024 + sl * 128 + (n16 >> 2) * 64 + g * 16), u32x4{pk(v[0], v[1]), tag, pk(v[2], v[3]), tag});
      if (lane < 4) *(volatile LAS unsigned*)(L + L_SY + lane * 1024 + 16 + a * 4) = 1u;
      STAMP(3);
    }
    for (unsigned it = 0; it < SPIN_CAP * 16; ++it) {
      if (syn[4] & syn[5]) break;
      __builtin_amdgcn_s_sleep(1);
    }
    __builtin_amdgcn_s_sleep(QA_SLEEP);
    {
      unsigned pend = 0xffu;
      const unsigned qoff = WS_QA + (unsigned)(ab * 65536 + a * 2048 + lane * 16);
      for (unsigned it = 0; it < SPIN_CAP; ++it) {
        u32x4 qv[8][2];
        fence_c();
#pragma unroll
        for (int i = 0; i < 8; ++i)
          if (pend & (1u << i)) { qv[i][0] = ld16c(rws, qoff + i * 8192); qv[i][1] = ld16c(rws, qoff + i * 8192 + 1024); }
#pragma unroll
        for (int i = 0; i < 8; ++i)
          if (pend & (1u << i)) {
            LR16(L_Q + (4 * i + a) * 1024 + ((lane ^ ((a << 2) | (i & 3))) << 4)) = u32x4{qv[i][0][0], qv[i][0][2], qv[i][1][0], qv[i][1][2]};
            const unsigned bad = (qv[i][0][1] ^ tag) | (qv[i][0][3] ^ tag) | (qv[i][1][1] ^ tag) | (qv[i][1][3] ^ tag);
            if (!__any(bad != 0u)) pend &= ~(1u << i);
          }
        if (it == 0) STAMP(4);
        if (!pend) break;
        __builtin_amdgcn_s_sleep(4);
      }
    }
    fence_c();
    STAMP(6);
    PV_ADDR(a, 1)
    bar();
    u32x4 va[2][8];
    if (AUXF == 0) { PV_FRAGS(0) PV_FRAGS(1) }
    sb0();
    if (blk == 0 && a == 0 && lane == 0) __builtin_amdgcn_raw_buffer_store_b32(tag, rws, WS_EPOCH, 0, 16);
    unsigned touch = 0;
    u32x4 uv[4], wor[8];
    {
      const rsrc_t rv = mk(wuv, 512 * 4096 * 2), ro = mk(wo, 4096 * 512 * 2);
#pragma unroll
      for (int i = 0; i < 4; ++i) uv[i] = ld16n(rv, (unsigned)((64 * sl + 16 * a + 4 * i + g) * 4096 + h * 128 + n16 * 8) * 2);
#pragma unroll
      for (int e = 0; e < 8; ++e) wor[e] = ld16n(ro, (unsigned)((h * 128 + sl * 16 + (g & 1) * 8 + e) * 512 + 128 * a + n16 * 8) * 2);
    }
    sb0();
    if (AUXF == 1) { PV_FRAGS(0) PV_FRAGS(1) }
    if (AUXF == 4) { PV_FRAGS(0) }
    sb0();
    asm volatile("s_barrier" ::: "memory");
    if (AUXF == 2) { PV_FRAGS(0) PV_FRAGS(1) }
    sb0();
    asm volatile("s_barrier" ::: "memory");
    if (AUXF == 3) { PV_FRAGS(0) PV_FRAGS(1) }
    if (AUXF == 4) { PV_FRAGS(1) }
    sb0();
    const char* p0;
    {
      const int xc = blk & 7, id = blk >> 3;
      const unsigned og = (unsigned)((id * 16 + (lane >> 2)) * (DF * 2) + xc * 352 + (lane & 3) * 116);
      const unsigned od = (unsigned)(xc * 176 * 1024 + (id * 44 + (a & 1) * 22 + min(lane, 21)) * 128);
      p0 = (const char*)(a == 0 ? wg : a == 1 ? wu : wd) + min(a < 2 ? og : od, 512u * DF * 2 - 4u);
    }
#define do_touch() asm volatile("global_load_dword %0, %1, off" : "+v"(touch) : "v"(p0) : "memory")
    if (TOUCH == 1) do_touch();
    bar();
    {
      u32x4 pf[8];
      pv_store(L, rws, va, pf, sw, sr, so, lane);
    }
#pragma unroll
    for (int i = 0; i < 4; ++i) LR16(L_UV + (16 * a + 4 * i + g) * 272 + n16 * 16) = uv[i];
    asm volatile("s_waitcnt vmcnt(0)" : "+v"(touch)::"memory");
    bar();
    if (TOUCH == 2) do_touch();
    u32x4 vf[2][2], of[8];
#pragma unroll
    for (int tt = 0; tt < 2; ++tt)
#pragma unroll
      for (int s = 0; s < 2; ++s) {
        const unsigned ad = L_UV + (32 * s + 8 * g + (n16 >> 2)) * 272 + a * 64 + tt * 32 + (lane & 3) * 8;
        const u32x2 v0 = TR(ad), v1 = TR(ad + 4 * 272);
        vf[tt][s] = u32x4{v0[0], v0[1], v1[0], v1[1]};
        asm volatile("" : "+v"(vf[tt][s]));
      }
#pragma unroll
    for (int j = 0; j < 8; ++j) { of[j] = colfrag(wor, j); asm volatile("" : "+v"(of[j])); }
    unsigned xo = WS_CX + (unsigned)(((h * 8 + 2 * a + ((n16 >> 2) & 1)) * 8 + sl) * 128 + (lane & 3) * 32 + g * 8);
    unsigned xi = WS_CX + (unsigned)(((h * 8 + sl) * 8 + (g >> 1)) * 128 + (n16 >> 2) * 32 + (g & 1) * 16);
    unsigned po = WS_P2 + (unsigned)(g * 8 + 2 * a + (n16 >> 3)) * PST + (unsigned)(blk * 128 + (n16 & 7) * 16);
    asm volatile("" : "+v"(xo), "+v"(xi), "+v"(po));
    STAMP(5);
    bar();
    if (TOUCH == 3) do_touch();
    {
      const u32x4 c0 = LR16(L_CC + (lane & 3) * 128 + g * 16), c1 = LR16(L_CC + (lane & 3) * 128 + 64 + g * 16);
      const f32x4 d0 = mfma16(vf[0][1], c1, mfma16(vf[0][0], c0, f32x4{0.f, 0.f, 0.f, 0.f}));
      const f32x4 d1 = mfma16(vf[1][1], c1, mfma16(vf[1][0], c0, f32x4{0.f, 0.f, 0.f, 0.f}));
      const f32x4 d = (n16 & 4) ? d1 : d0;
      if (n16 < 8) st8c(rws, xo, u32x2{canon2(pk(d[0], d[1])), canon2(pk(d[2], d[3]))});
      u32x4 cf[4];
      const unsigned xis[4] = {xi, xi + 256, xi + 512, xi + 768};
      __builtin_amdgcn_s_sleep(CX_SLEEP);
      poll16<4, 0>(rws, xis, cf);
      if (TOUCH > 1) asm volatile("s_waitcnt vmcnt(0)" : "+v"(touch)::"memory");
      STAMP(7);
      f32x4 o[8];
#pragma unroll
      for (int j = 0; j < 8; ++j) o[j] = f32x4{0.f, 0.f, 0.f, 0.f};
#pragma unroll
      for (int s = 0; s < 4; ++s)
#pragma unroll
        for (int j = 0; j < 8; ++j) o[j] = mfma16(cf[s], of[j], o[j]);
      st16c(rws, po, u32x4{canon2(pk(o[0][0], o[1][0])), canon2(pk(o[2][0], o[3][0])), canon2(pk(o[4][0], o[5][0])), canon2(pk(o[6][0], o[7][0]))});
    }
    STAMP(8);
    if (ffn) {
      u32x4 gu[8], dn[8], gf[2][4], df[8];
      {
        const rsrc_t rg = mk(wg, 512 * DF * 2), ru = mk(wu, 512 * DF * 2), rd = mk(wd, DF * 512 * 2);
        const unsigned go = (unsigned)((a * 128 + (lane >> 1)) * DF + fi * 16 + (lane & 1) * 8) * 2;
#pragma unroll
        for (int i = 0; i < 8; ++i) gu[i] = ld16(i < 4 ? rg : ru, go + (i & 3) * (32 * DF * 2));
#pragma unroll
        for (int e = 0; e < 8; ++e) dn[e] = ld16(rd, (unsigned)((fi * 16 + (g & 1) * 8 + e) * 512 + a * 128 + n16 * 8) * 2);
        fence_c();
      }
      LAS float* red = (LAS float*)(L + L_RED + a * 704);
      float csum[2];
      {
        const unsigned sb = L_GU + a * 8192;
#pragma unroll
        for (int i = 0; i < 8; ++i) LR16(sb + i * 1024 + lane * 16) = gu[i];
        const u32x4 ones = {0x3f803f80u, 0x3f803f80u, 0x3f803f80u, 0x3f803f80u};
#pragma unroll
        for (int m = 0; m < 2; ++m) {
          f32x4 c = {0.f, 0.f, 0.f, 0.f};
#pragma unroll
          for (int s = 0; s < 4; ++s) {
            const unsigned ad = sb + m * 4096 + (32 * s + 8 * g + (n16 >> 2)) * 32 + (lane & 3) * 8;
            const u32x2 v0 = TR(ad), v1 = TR(ad + 128);
            gf[m][s] = u32x4{v0[0], v0[1], v1[0], v1[1]};
            c = mfma16(ones, gf[m][s], c);
            asm volatile("" : "+v"(gf[m][s]));
          }
          csum[m] = c[0];
        }
#pragma unroll
        for (int j = 0; j < 8; ++j) { df[j] = colfrag(dn, j); asm volatile("" : "+v"(df[j])); }
      }
      if (g < 2) red[128 + g * 16 + n16] = g ? csum[1] : csum[0];
      STAMP(12);
      u32x4 hv[4];
      {
        const unsigned ho = WS_HB + (unsigned)((blk & 7) * 4096 + ((n16 >> 2) * 512 + a * 128 + g * 8) * 2);
        const unsigned hos[4] = {ho, ho + 64, ho + 128, ho + 192};
        poll16<4, 1>(rws, hos, hv);
      }
      STAMP(9);
      {
        f32x4 G = {0.f, 0.f, 0.f, 0.f}, U = {0.f, 0.f, 0.f, 0.f};
        float s1 = 0.f, s2 = 0.f;
#pragma unroll
        for (int s = 0; s < 4; ++s) {
          G = mfma16(hv[s], gf[0][s], G); U = mfma16(hv[s], gf[1][s], U);
#pragma unroll
          for (int e = 0; e < 4; ++e) { s1 = dot2(hv[s][e], 0x3f803f80u, s1); s2 = dot2(hv[s][e], hv[s][e], s2); }
        }
        s1 = sum32(sum16(s1)); s2 = sum32(sum16(s2));
        red[lane] = G[0];
        red[64 + lane] = U[0];
        if (lane < 16) { red[160 + (lane >> 2) * 2] = s1; red[161 + (lane >> 2) * 2] = s2; }
      }
      bar();
      float G = 0.f, U = 0.f, cg = 0.f, cu = 0.f, s1 = 0.f, s2 = 0.f;
#pragma unroll
      for (int k = 0; k < 4; ++k) {
        LAS float* rk = (LAS float*)(L + L_RED + k * 704);
        G += rk[lane]; U += rk[64 + lane]; cg += rk[128 + n16]; cu += rk[144 + n16];
        s1 += rk[160 + g * 2]; s2 += rk[161 + g * 2];
      }
      const float mean = s1 * (1.f / 512.f);
      const float var = fmaxf(s2 * (1.f / 512.f) - mean * mean, 0.f);
      const float rstd = __builtin_amdgcn_rsqf(var + 1e-5f);
      const float gg = (G - mean * cg) * rstd, uu = (U - mean * cu) * rstd;
      const float z = 0.7978845608028654f * (gg + 0.044715f * gg * gg * gg);
      const float th = 1.f - 2.f * __builtin_amdgcn_rcpf(1.f + __expf(2.f * z));
      const float hh = 0.5f * gg * (1.f + th) * uu;
      *(LAS uint16_t*)(L + L_HS + a * 128 + lane * 2) = (uint16_t)pk(hh, 0.f);
      fence_c();
      u32x4 ha = LR16(L_HS + a * 128 + (n16 >> 2) * 32 + (g & 1) * 16);
      if (g >= 2) ha = u32x4{0u, 0u, 0u, 0u};
      float o[8];
#pragma unroll
      for (int j = 0; j < 8; ++j) o[j] = mfma16(ha, df[j], f32x4{0.f, 0.f, 0.f, 0.f})[0];
      st16c(rws, WS_P4 + (unsigned)(((g * 8 + 2 * a + (n16 >> 3)) * NFFN + fi) * 128 + (n16 & 7) * 16),
            u32x4{canon2(pk(o[0], o[1])), canon2(pk(o[2], o[3])), canon2(pk(o[4], o[5])), canon2(pk(o[6], o[7]))});
      STAMP(10);
    }
    STAMP_FLUSH;
  }
}

extern "C" void kernel_launch(void* const* d_in, const int* in_sizes, int n_in, void* d_out, int out_size, void* d_ws,
                              size_t ws_size, hipStream_t stream) {
  fused_kernel<<<256, 512, 0, stream>>>((const uint16_t*)d_in[0], (const uint16_t*)d_in[1], (const uint16_t*)d_in[2],
                                        (const uint16_t*)d_in[3], (const uint16_t*)d_in[4], (const uint16_t*)d_in[5],
                                        (const uint16_t*)d_in[7], (const uint16_t*)d_in[8], (const uint16_t*)d_in[9],
                                        (uint16_t*)d_out, (char*)d_ws);
}
```

```cpp
#include <hip/hip_runtime.h>
#include <hip/hip_bf16.h>
#include <stdint.h>

typedef __bf16 bf16x8 __attribute__((ext_vector_type(8)));
typedef __bf16 bf16x2 __attribute__((ext_vector_type(2)));
typedef short s16x4 __attribute__((ext_vector_type(4)));
typedef float f32x2 __attribute__((ext_vector_type(2)));
typedef float f32x4 __attribute__((ext_vector_type(4)));
typedef float f32x16 __attribute__((ext_vector_type(16)));
typedef unsigned u32x2 __attribute__((ext_vector_type(2)));
typedef unsigned u32x4 __attribute__((ext_vector_type(4)));
typedef __amdgpu_buffer_rsrc_t rsrc_t;
#define DI __device__ __forceinline__
#define LAS __attribute__((address_space(3)))

#define CKV_WIN 4
#define WPOL 0
#define TPOL ""
#define QA_SLEEP 28
#define QX_PRE 8
#define QX_SLEEP 4
#define CX_SLEEP 16
#define ML_SLEEP 2

constexpr int DF = 1376;
constexpr int NFFN = 86;
constexpr unsigned PST = 32768 + 256;
constexpr unsigned WS_EPOCH = 0;
constexpr unsigned WS_QA = 4096;
constexpr unsigned WS_QX = WS_QA + 262144;
constexpr unsigned WS_CLR = WS_QX + 524288;
constexpr unsigned WS_ML = WS_CLR;
constexpr unsigned WS_HB = WS_ML + 65536;
constexpr unsigned WS_CX = WS_HB + 32768;
constexpr unsigned WS_P4 = WS_CX + 262144;
constexpr unsigned WS_P2 = WS_P4 + 32 * NFFN * 128;
constexpr unsigned CLR_PER_WG = 7168;
static_assert(WS_P2 + 32 * PST <= WS_HB + 256 * CLR_PER_WG, "clear span");
constexpr unsigned WS_PART = WS_HB + 256 * CLR_PER_WG;
constexpr unsigned WS_DBG = WS_PART + 256 * PST;
constexpr unsigned WS_END = WS_DBG + 256 * 8 * 16 * 8;
constexpr unsigned SENT = 0xFFFFFFFFu;
constexpr unsigned SPIN_CAP = 1u << 18;

constexpr unsigned L_Q = 131072;
constexpr unsigned L_WK = L_Q;
constexpr unsigned L_QP = L_Q + 16384;
constexpr unsigned L_SY = L_Q + 28 * 1024;
constexpr unsigned L_P = L_Q;
constexpr unsigned L_MX = L_Q + 8192;
constexpr unsigned L_LS = L_Q + 9216;
constexpr unsigned L_OS = L_Q + 12288;
constexpr unsigned L_CC = L_Q + 28672;
constexpr unsigned L_RED = L_Q + 8192;
constexpr unsigned L_HS = L_Q + 30720;
constexpr unsigned L_UV = 0;
constexpr unsigned L_GU = 32768;
constexpr unsigned L_RS = 65536;

#define STAMP(i) do {} while (0)
#define STAMP_DECL
#define STAMP_FLUSH

DI rsrc_t mk(const void* p, unsigned bytes) { return __builtin_amdgcn_make_buffer_rsrc((void*)p, 0, bytes, 0x00020000); }
DI u32x4 ld16(rsrc_t r, unsigned off) { return __builtin_amdgcn_raw_buffer_load_b128(r, off, 0, WPOL); }
DI u32x4 ld16n(rsrc_t r, unsigned off) { return __builtin_amdgcn_raw_buffer_load_b128(r, off, 0, 2); }
DI u32x4 ld16c(rsrc_t r, unsigned off) { return __builtin_amdgcn_raw_buffer_load_b128(r, off, 0, 16); }
DI u32x2 ld8c(rsrc_t r, unsigned off) { return __builtin_amdgcn_raw_buffer_load_b64(r, off, 0, 16); }
DI void st16c(rsrc_t r, unsigned off, u32x4 v) { __builtin_amdgcn_raw_buffer_store_b128(v, r, off, 0, 16); }
DI void st8c(rsrc_t r, unsigned off, u32x2 v) { __builtin_amdgcn_raw_buffer_store_b64(v, r, off, 0, 16); }
DI void fence_c() { asm volatile("" ::: "memory"); }
DI void bar() { asm volatile("s_waitcnt lgkmcnt(0)\n\ts_barrier" ::: "memory"); }
DI void drain() { asm volatile("s_waitcnt vmcnt(0)" ::: "memory"); }
template <int N> DI void wait_vm() { asm volatile("s_waitcnt vmcnt(%0)" ::"n"(N) : "memory"); }
template <bool NT> DI void dma16(const void* sbase, unsigned voff, unsigned lds_dst) {
  unsigned keep;
  if (NT)
    asm volatile("s_mov_b32 %0, m0\n\ts_mov_b32 m0, %3\n\ts_nop 0\n\tglobal_load_lds_dwordx4 %1, %2 nt\n\ts_mov_b32 m0, %0"
                 : "=&s"(keep) : "v"(voff), "s"(sbase), "s"(lds_dst) : "memory");
  else
    asm volatile("s_mov_b32 %0, m0\n\ts_mov_b32 m0, %3\n\ts_nop 0\n\tglobal_load_lds_dwordx4 %1, %2\n\ts_mov_b32 m0, %0"
                 : "=&s"(keep) : "v"(voff), "s"(sbase), "s"(lds_dst) : "memory");
}
DI float bflo(unsigned u) { return __uint_as_float(u << 16); }
DI float bfhi(unsigned u) { return __uint_as_float(u & 0xffff0000u); }
DI unsigned pk(float a, float b) { f32x2 f = {a, b}; return __builtin_bit_cast(unsigned, __builtin_convertvector(f, bf16x2)); }
DI unsigned canon(float v) { unsigned u = __float_as_uint(v); return u == SENT ? 0x7fc00000u : u; }
DI unsigned canon2(unsigned u) { return u == SENT ? 0x7fc07fc0u : u; }
DI float dot2(unsigned a, unsigned b, float c) { return __builtin_amdgcn_fdot2_f32_bf16(__builtin_bit_cast(bf16x2, a), __builtin_bit_cast(bf16x2, b), c, false); }
DI void sb0() { __builtin_amdgcn_sched_barrier(0); }
template <int C> DI float dppf(float x) { return __int_as_float(__builtin_amdgcn_update_dpp(0, __float_as_int(x), C, 0xf, 0xf, true)); }
DI float sum16(float x) { auto r = __builtin_amdgcn_permlane16_swap(__float_as_uint(x), __float_as_uint(x), false, false); return __uint_as_float(r[0]) + __uint_as_float(r[1]); }
DI float sum32(float x) { auto r = __builtin_amdgcn_permlane32_swap(__float_as_uint(x), __float_as_uint(x), false, false); return __uint_as_float(r[0]) + __uint_as_float(r[1]); }
DI float max16(float x) { auto r = __builtin_amdgcn_permlane16_swap(__float_as_uint(x), __float_as_uint(x), false, false); return fmaxf(__uint_as_float(r[0]), __uint_as_float(r[1])); }
DI float max32(float x) { auto r = __builtin_amdgcn_permlane32_swap(__float_as_uint(x), __float_as_uint(x), false, false); return fmaxf(__uint_as_float(r[0]), __uint_as_float(r[1])); }
DI f32x4 mfma16(u32x4 a, u32x4 b, f32x4 c) { return __builtin_amdgcn_mfma_f32_16x16x32_bf16(__builtin_bit_cast(bf16x8, a), __builtin_bit_cast(bf16x8, b), c, 0, 0, 0); }
DI f32x16 mfma32(u32x4 a, u32x4 b, f32x16 c) { return __builtin_amdgcn_mfma_f32_32x32x16_bf16(__builtin_bit_cast(bf16x8, a), __builtin_bit_cast(bf16x8, b), c, 0, 0, 0); }
DI float sel4(f32x4 v, int g) { float a = (g & 1) ? v[1] : v[0], b = (g & 1) ? v[3] : v[2]; return (g & 2) ? b : a; }
DI bool bad4(u32x4 v) { return v[0] == SENT || v[1] == SENT || v[2] == SENT || v[3] == SENT; }
DI u32x4 colfrag(const u32x4* r, int j) {
  const unsigned sel = (j & 1) ? 0x07060302u : 0x05040100u;
  u32x4 o;
#pragma unroll
  for (int e = 0; e < 4; ++e) o[e] = __builtin_amdgcn_perm(r[2 * e + 1][j >> 1], r[2 * e][j >> 1], sel);
  return o;
}

#define WL 0
#define P_POL 16
#define L_POL 18
#define PRE_PV 0
#define QA_L2 0
#define LATEC 1
#define PSENT 0
#define PV_SLEEP 0
#define HB_SL 1
#define P4_SLEEP 24
#define PENDP 0
#define R_SLEEP 66
#define AUXF 3
#define ATTF 1
#define TOUCH 4
#define MASKED 0
DI unsigned max4u(unsigned m, u32x4 v) { return max(max(max(m, v[0]), v[1]), max(v[2], v[3])); }
DI u32x4 pick(const u32x4 msk, unsigned ww) { return u32x4{msk[0] & ww, msk[1] & ww, msk[2] & ww, msk[3] & ww}; }
DI f32x4 fold(f32x4 d) {
#pragma unroll
  for (int r = 0; r < 4; ++r) {
    const unsigned t = __float_as_uint(dppf<0x128>(d[r]));
    d[r] += __uint_as_float(__builtin_amdgcn_permlane32_swap(t, t, false, false)[1]);
  }
  return d;
}
template <int N> DI void pollp(rsrc_t rws, const unsigned* off, u32x4* v) {
  unsigned pend = (1u << N) - 1u;
#pragma unroll
  for (int i = 0; i < N; ++i) v[i] = u32x4{SENT, SENT, SENT, SENT};
  for (unsigned it = 0; it < SPIN_CAP; ++it) {
    fence_c();
#pragma unroll
    for (int i = 0; i < N; ++i)
      if (pend & (1u << i)) v[i] = ld16c(rws, off[i]);
    sb0();
    pend = 0;
#pragma unroll
    for (int i = 0; i < N; ++i) pend |= __any(max4u(0u, v[i]) == SENT) ? 1u << i : 0u;
    if (!pend) break;
  }
  fence_c();
}
template <int N, int SL> DI void poll16(rsrc_t rws, const unsigned* off, u32x4* v) {
  bool bad[N];
#pragma unroll
  for (int i = 0; i < N; ++i) { v[i] = u32x4{SENT, SENT, SENT, SENT}; bad[i] = true; }
  for (unsigned it = 0; it < SPIN_CAP; ++it) {
    fence_c();
#pragma unroll
    for (int i = 0; i < N; ++i)
      if (MASKED ? bad[i] : true) v[i] = ld16c(rws, off[i]);
    bool any = false;
#pragma unroll
    for (int i = 0; i < N; ++i) { bad[i] = max4u(0u, v[i]) == SENT; any |= bad[i]; }
    if (!__any(any)) break;
    if (SL) __builtin_amdgcn_s_sleep(SL);
  }
  fence_c();
}
DI void pv_store(LAS char* L, rsrc_t rws, const u32x4 (*va)[8], u32x4* pf, const unsigned* sw, unsigned sr, unsigned so, int lane) {
#pragma unroll
  for (int ks = 0; ks < 8; ++ks) pf[ks] = *(LAS u32x4*)(L + L_P + ks * 1024 + lane * 16);
  f32x16 O[2];
#pragma unroll
  for (int t = 0; t < 2; ++t) {
#pragma unroll
    for (int i = 0; i < 16; ++i) O[t][i] = 0.f;
#pragma unroll
    for (int ks = 0; ks < 8; ++ks) O[t] = mfma32(va[t][ks], pf[ks], O[t]);
  }
#pragma unroll
  for (int i = 0; i < 4; ++i) {
    const int t = i >> 1, u = i & 1;
    *(LAS u32x4*)(L + sw[i]) = u32x4{pk(O[t][8 * u], O[t][8 * u + 1]), pk(O[t][8 * u + 2], O[t][8 * u + 3]), pk(O[t][8 * u + 4], O[t][8 * u + 5]), pk(O[t][8 * u + 6], O[t][8 * u + 7])};
  }
  u32x4 v[4];
#pragma unroll
  for (int i = 0; i < 4; ++i) v[i] = *(LAS u32x4*)(L + sr + i * 1024);
  sb0();
#pragma unroll
  for (int i = 0; i < 4; ++i) {
    if (PSENT) v[i] = u32x4{min(v[i][0], SENT - 1u), min(v[i][1], SENT - 1u), min(v[i][2], SENT - 1u), min(v[i][3], SENT - 1u)};
    __builtin_amdgcn_raw_buffer_store_b128(v[i], rws, so, i * 64 * PST, P_POL);
  }
  sb0();
}
template <int N> DI f32x4 line_sum(rsrc_t rws, unsigned base, int first, int count, int lane, const u32x4 msk) {
  u32x4 v[N], one[N];
  unsigned off[N];
#pragma unroll
  for (int i = 0; i < N; ++i) {
    const int p = first + 8 * i + (lane >> 3);
    off[i] = base + (unsigned)((p < count ? p : 0) * 128 + (lane & 7) * 16);
    one[i] = pick(msk, p < count ? 0x3f803f80u : 0u);
  }
  if (PENDP) pollp<N>(rws, off, v); else poll16<N, 0>(rws, off, v);
  f32x4 d[2] = {{0.f, 0.f, 0.f, 0.f}, {0.f, 0.f, 0.f, 0.f}};
#pragma unroll
  for (int i = 0; i < N; ++i) d[i & 1] = mfma16(one[i], v[i], d[i & 1]);
  return fold(d[0] + d[1]);
}

__global__ void __launch_bounds__(512, 2)
fused_kernel(const uint16_t* __restrict__ x, const uint16_t* __restrict__ ckv, const uint16_t* __restrict__ wq,
             const uint16_t* __restrict__ wuk, const uint16_t* __restrict__ wuv, const uint16_t* __restrict__ wo,
             const uint16_t* __restrict__ wg, const uint16_t* __restrict__ wu, const uint16_t* __restrict__ wd,
             uint16_t* __restrict__ out, char* __restrict__ ws) {
  __shared__ __attribute__((aligned(16))) char lds[163840];
  LAS char* L = (LAS char*)lds;
  const unsigned lbase = (unsigned)(size_t)L;
  const int blk = blockIdx.x;
  const int lane = threadIdx.x & 63;
  const int wave = __builtin_amdgcn_readfirstlane(threadIdx.x >> 6);
  const int g = lane >> 4, n16 = lane & 15;
  const rsrc_t rws = mk(ws, WS_END);
  const int h = (blk & 7) * 4 + ((blk >> 3) & 3), sl = blk >> 5;
  const int ab = (blk & 7) >> 1, aj = (blk & 1) * 32 + (blk >> 3);
  const int fi = (blk & 7) * 11 + (blk >> 3);
  const bool ffn = (blk >> 3) < 11 && fi < NFFN;
#define LR16(off) (*(LAS u32x4*)(L + (off)))
#define TR(off) __builtin_bit_cast(u32x2, __builtin_amdgcn_ds_read_tr16_b64_v4i16((LAS s16x4*)(L + (off))))
#define PV_ADDR(wv, pr) \
    unsigned to[2][2][2], sw[4], sr = 65536 + ((wv) * 2 + (pr)) * 4096 + (lane >> 3) * 128 + (((lane ^ (lane >> 3)) & 7) << 4); \
    unsigned so = WS_PART + (unsigned)((lane >> 3) * 8 + 2 * (wv) + (pr)) * PST + (unsigned)((ab * 64 + aj) * 128 + (lane & 7) * 16); \
    _Pragma("unroll") for (int i = 0; i < 8; ++i) { \
      const int k = i >> 2, tt = (i >> 1) & 1, u = i & 1, q = n16 >> 2, p = lane & 3; \
      to[k][tt][u] = (64 * u + 8 * tt + 4 * hf + q) * 1024 + (wv) * 256 + (((8 * (pr) + 4 * k + 2 * (p & 1) + (g & 1)) ^ ((q << 2) | ((2 * tt + hf) & 3))) << 4) + (p >> 1) * 8; \
      asm volatile("" : "+v"(to[k][tt][u])); } \
    _Pragma("unroll") for (int i = 0; i < 4; ++i) { \
      sw[i] = 65536 + ((wv) * 2 + (pr)) * 4096 + r32 * 128 + ((((i >> 1) * 4 + hf * 2 + (i & 1)) ^ (r32 & 7)) << 4); asm volatile("" : "+v"(sw[i])); } \
    asm volatile("" : "+v"(sr), "+v"(so));
#define PV_FRAGS(k) \
    _Pragma("unroll") for (int ks = 0; ks < 8; ++ks) _Pragma("unroll") for (int tt = 0; tt < 2; ++tt) { \
      const u32x2 v_ = TR(to[k][tt][ks >> 2] + (ks & 3) * 16384); va[k][ks][2 * tt] = v_[0]; va[k][ks][2 * tt + 1] = v_[1]; }
  const int r32 = lane & 31, hf = lane >> 5;
  STAMP_DECL
  STAMP(0);

  if (wave < 4) {
    const int w = wave;
    unsigned touch = 0;
    if (PSENT == 2) {
#pragma unroll
      for (int i = 0; i < 8; ++i) st16c(rws, WS_PART + blk * PST + (w * 8 + i) * 1024 + lane * 16, u32x4{SENT, SENT, SENT, SENT});
    }
    bar();
    STAMP(9);
    if (PSENT == 1) {
#pragma unroll
      for (int i = 0; i < 8; ++i) st16c(rws, WS_PART + blk * PST + (w * 8 + i) * 1024 + lane * 16, u32x4{SENT, SENT, SENT, SENT});
    }
    if (w == 0) {
      if (!LATEC) {
#pragma unroll
        for (int i = 0; i < 7; ++i) st16c(rws, WS_HB + blk * CLR_PER_WG + i * 1024 + lane * 16, u32x4{SENT, SENT, SENT, SENT});
      }
    }
    {
      const char* src = (const char*)ckv + ((size_t)(ab * 8192 + aj * 128 + w * 32) << 10);
#pragma unroll
      for (int r = 0; r < 32; ++r) {
        if (r >= CKV_WIN) wait_vm<CKV_WIN - 1>();
        const unsigned f = ((r & 3) << 2) | ((r >> 2) & 3);
        dma16<true>(src, r * 1024 + ((lane ^ f) << 4), lbase + (w * 32 + r) * 1024);
      }
    }
    unsigned qo[8];
    u32x4 msk;
#pragma unroll
    for (int s = 0; s < 8; ++s) qo[s] = (w * 32 + r32) * 1024 + (((2 * s + hf) ^ (((r32 & 3) << 2) | ((r32 >> 2) & 3))) << 4);
    PV_ADDR(w, 0)
    drain();
    asm volatile("" : "+v"(touch));
    STAMP(1);
    u32x4 ca[32];
#pragma unroll
    for (int s = 0; s < 32; ++s) ca[s] = LR16(qo[s & 7] + (s >> 3) * 256);
#pragma unroll
    for (int s = 0; s < 8; ++s) { qo[s] += L_Q - w * 32768; asm volatile("" : "+v"(qo[s])); }
    bar();
    STAMP(2);
    f32x16 S;
#pragma unroll
    for (int i = 0; i < 16; ++i) S[i] = 0.f;
    u32x4 va[2][8];
    {
      u32x4 qb[4][8];
#pragma unroll
      for (int s = 0; s < 8; ++s) qb[0][s] = LR16(qo[s]);
#pragma unroll
      for (int k = 0; k < 4; ++k) {
        if (k < 3) {
#pragma unroll
          for (int s = 0; s < 8; ++s) qb[k + 1][s] = LR16(qo[s] + (k + 1) * 256);
        }
        sb0();
#pragma unroll
        for (int s = 0; s < 8; ++s) S = mfma32(ca[8 * k + s], qb[k][s], S);
        sb0();
        if (ATTF == 0 && k == 1) { PV_FRAGS(0) }
        if (ATTF == 0 && k == 2) { PV_FRAGS(1) }
      }
      sb0();
    }
    float mx = S[0];
#pragma unroll
    for (int i = 1; i < 16; ++i) mx = fmaxf(mx, S[i]);
    bar();
    *(LAS float*)(L + L_MX + r32 * 32 + (w * 2 + hf) * 4) = mx;
    bar();
    STAMP(13);
    {
      const f32x4 a = *(LAS f32x4*)(L + L_MX + r32 * 32), b = *(LAS f32x4*)(L + L_MX + r32 * 32 + 16);
      sb0();
      if (ATTF == 1) { PV_FRAGS(0) PV_FRAGS(1) }
      sb0();
      mx = fmaxf(fmaxf(fmaxf(a[0], a[1]), fmaxf(a[2], a[3])), fmaxf(fmaxf(b[0], b[1]), fmaxf(b[2], b[3])));
    }
#pragma unroll
    for (int s = 0; s < 2; ++s) {
      u32x4 p;
#pragma unroll
      for (int e = 0; e < 4; ++e) p[e] = pk(__builtin_amdgcn_exp2f(S[8 * s + 2 * e] - mx), __builtin_amdgcn_exp2f(S[8 * s + 2 * e + 1] - mx));
      LR16(L_P + (w * 2 + s) * 1024 + lane * 16) = p;
    }
    bar();
    STAMP(10);
    float lsum;
    {
      u32x4 pf[8];
      pv_store(L, rws, va, pf, sw, sr, so, lane);
      const u32x4 ones = {0x3f803f80u, 0x3f803f80u, 0x3f803f80u, 0x3f803f80u};
      f32x16 R;
#pragma unroll
      for (int i = 0; i < 16; ++i) R[i] = 0.f;
#pragma unroll
      for (int ks = 0; ks < 8; ++ks) R = mfma32(ones, pf[ks], R);
      lsum = R[0];
    }
    STAMP(3);
#pragma unroll
    for (int e = 0; e < 4; ++e) msk[e] = ((n16 & 7) >> 1) == e ? ((n16 & 1) ? 0xffff0000u : 0xffffu) : 0u;
    if (!PSENT) drain();
    bar();
    if (w == 0 && lane < 32) { u32x2 v = {canon(mx), canon(lsum)}; st8c(rws, WS_ML + (unsigned)((ab * 64 + aj) * 32 + r32) * 8, v); }
    STAMP(4);
    u32x2 ml = {SENT, SENT};
    u32x4 pv[8];
    unsigned pvo = WS_PART + (unsigned)(h * 8 + sl) * PST + (unsigned)(w * 8192 + lane * 16), got = 0;
#pragma unroll
    for (int i = 0; i < 8; ++i) pv[i] = u32x4{0u, 0u, 0u, 0u};
    for (unsigned it = 0; it < SPIN_CAP; ++it) {
      __builtin_amdgcn_s_sleep(ML_SLEEP);
      fence_c();
      if (ml[0] == SENT || ml[1] == SENT) ml = ld8c(rws, WS_ML + (unsigned)((w * 64 + lane) * 32 + h) * 8);
      const unsigned long long miss = __ballot(ml[0] == SENT || ml[1] == SENT);
      if (PRE_PV && __popcll(miss) <= PRE_PV) {
        fence_c();
#pragma unroll
        for (int i = 0; i < 8; ++i)
          if (!((got >> i) & 1u) && !((miss >> (8 * i)) & 0xffull)) { pv[i] = ld16c(rws, pvo + i * 1024); got |= 1u << i; }
      }
      if (!miss) break;
    }
    fence_c();
    STAMP(5);
    if (!PRE_PV) {
#pragma unroll
      for (int i = 0; i < 8; ++i) pv[i] = __builtin_amdgcn_raw_buffer_load_b128(rws, pvo + i * 1024, 0, L_POL);
    }
    fence_c();
    {
      const float m = __uint_as_float(ml[0]);
      float M = m;
      M = fmaxf(M, dppf<0x128>(M)); M = fmaxf(M, dppf<0x124>(M)); M = fmaxf(M, dppf<0x122>(M)); M = fmaxf(M, dppf<0x121>(M));
      M = max16(M); M = max32(M);
      const float we = __builtin_amdgcn_exp2f(m - M);
      const unsigned wp = pk(we, we);
      float l = bfhi(wp) * __uint_as_float(ml[1]);
      l += dppf<0x128>(l); l += dppf<0x124>(l); l += dppf<0x122>(l); l += dppf<0x121>(l);
      l = sum16(l); l = sum32(l);
      float inv = 1.f / l;
      asm volatile("" : "+v"(inv));
      u32x4 wb[8];
#pragma unroll
      for (int i = 0; i < 8; ++i) {
        wb[i] = pick(msk, (unsigned)__builtin_amdgcn_ds_bpermute((8 * i + (lane >> 3)) * 4, (int)wp));
        asm volatile("" : "+v"(wb[i]));
      }
      sb0();
      if (PSENT) {
        for (unsigned it = 0; it < SPIN_CAP; ++it) {
          unsigned mm = 0;
#pragma unroll
          for (int i = 0; i < 8; ++i) mm = max4u(mm, pv[i]);
          if (!__any(mm == SENT)) break;
          asm volatile("" : "+v"(pvo)::"memory");
#pragma unroll
          for (int i = 0; i < 8; ++i) pv[i] = ld16c(rws, pvo + i * 1024);
        }
        fence_c();
      }
      f32x4 d[2] = {{0.f, 0.f, 0.f, 0.f}, {0.f, 0.f, 0.f, 0.f}};
#pragma unroll
      for (int i = 0; i < 8; ++i) d[i & 1] = mfma16(wb[i], pv[i], d[i & 1]);
      const f32x4 c = fold(d[0] + d[1]);
      if (g < 2 && n16 < 8) *(LAS u32x2*)(L + L_CC + w * 128 + n16 * 16 + g * 8) = u32x2{pk(c[0] * inv, c[1] * inv), pk(c[2] * inv, c[3] * inv)};
    }
    STAMP(6);
    bar();
    if (blk >= 224) {
      const int lc = blk - 224;
      const u32x4 xr = *(const u32x4*)(x + lc * 64 + (lane & 7) * 8);
      LAS f32x4* rs = (LAS f32x4*)(L + L_RS + w * 256 + n16 * 32 + g * 16);
      __builtin_amdgcn_s_sleep(R_SLEEP);
      f32x4 acc = line_sum<8>(rws, WS_P2 + lc * PST, w * 64, 256, lane, msk);
      STAMP(7);
      if (g < 2 && n16 < 8) rs[0] = acc;
      bar();
      u32x4 hb;
      f32x4 t[8];
      if (w == 0) {
        f32x4 lo = {bflo(xr[0]), bfhi(xr[0]), bflo(xr[1]), bfhi(xr[1])}, hi = {bflo(xr[2]), bfhi(xr[2]), bflo(xr[3]), bfhi(xr[3])};
#pragma unroll
        for (int k = 0; k < 8; ++k) t[k] = *(LAS f32x4*)(L + L_RS + (k >> 1) * 256 + (lane & 7) * 32 + (k & 1) * 16);
        sb0();
        lo += (t[0] + t[2]) + (t[4] + t[6]); hi += (t[1] + t[3]) + (t[5] + t[7]);
        hb = u32x4{pk(lo[0], lo[1]), pk(lo[2], lo[3]), pk(hi[0], hi[1]), pk(hi[2], hi[3])};
        st16c(rws, WS_HB + (unsigned)((lane >> 3) * 4096 + lc * 128 + (lane & 7) * 16), u32x4{canon2(hb[0]), canon2(hb[1]), canon2(hb[2]), canon2(hb[3])});
      }
      STAMP(8);
      __builtin_amdgcn_s_sleep(P4_SLEEP);
      acc = line_sum<3>(rws, WS_P4 + lc * (NFFN * 128), w * 24, NFFN, lane, msk);
      STAMP(11);
      if (g < 2 && n16 < 8) rs[64] = acc;
      bar();
      if (w == 0 && lane < 8) {
        f32x4 lo = {bflo(hb[0]), bfhi(hb[0]), bflo(hb[1]), bfhi(hb[1])}, hi = {bflo(hb[2]), bfhi(hb[2]), bflo(hb[3]), bfhi(hb[3])};
#pragma unroll
        for (int k = 0; k < 8; ++k) t[k] = *(LAS f32x4*)(L + L_RS + 1024 + (k >> 1) * 256 + lane * 32 + (k & 1) * 16);
        sb0();
        lo += (t[0] + t[2]) + (t[4] + t[6]); hi += (t[1] + t[3]) + (t[5] + t[7]);
        *(u32x4*)(out + lc * 64 + lane * 8) = u32x4{pk(lo[0], lo[1]), pk(lo[2], lo[3]), pk(hi[0], hi[1]), pk(hi[2], hi[3])};
      }
      STAMP(12);
    }
    STAMP_FLUSH;
  } else {
    const int a = wave - 4;
    volatile LAS unsigned* syn = (volatile LAS unsigned*)(L + L_SY + a * 1024);
    const unsigned tag = __builtin_amdgcn_raw_buffer_load_b32(rws, WS_EPOCH, 0, 16) + 1u;
    if (lane < 12) syn[lane] = 0u;
    if (a == 0) {
      u32x4 xs[4], xa[2], wr[2][8];
      const rsrc_t rx = mk(x, 4096), rq = mk(wq, 512 * 4096 * 2);
#pragma unroll
      for (int i = 0; i < 4; ++i) xs[i] = __builtin_amdgcn_raw_buffer_load_b128(rx, (n16 >> 2) * 1024 + (g * 4 + (n16 & 3)) * 64 + i * 16, 0, 0);
#pragma unroll
      for (int s = 0; s < 2; ++s) xa[s] = __builtin_amdgcn_raw_buffer_load_b128(rx, (n16 >> 2) * 1024 + (64 * sl + 32 * s + 8 * g) * 2, 0, 0);
      const unsigned qrow = (unsigned)((64 * sl + 8 * g) * 4096 + h * 128 + n16 * 8) * 2;
#pragma unroll
      for (int s = 0; s < 2; ++s)
#pragma unroll
        for (int e = 0; e < 8; ++e) wr[s][e] = __builtin_amdgcn_raw_buffer_load_b128(rq, qrow, (32 * s + e) * 8192, 2);
      bar();
      float s1 = 0.f, s2 = 0.f;
      const unsigned one2 = 0x3f803f80u;
#pragma unroll
      for (int i = 0; i < 4; ++i)
#pragma unroll
        for (int e = 0; e < 4; ++e) {
          s1 = dot2(xs[i][e], one2, s1); s2 = dot2(xs[i][e], xs[i][e], s2);
        }
      s1 += dppf<0xb1>(s1); s2 += dppf<0xb1>(s2); s1 += dppf<0x4e>(s1); s2 += dppf<0x4e>(s2);
      s1 = sum32(sum16(s1)); s2 = sum32(sum16(s2));
      const float mean = s1 * (1.f / 512.f);
      const float rstd = __builtin_amdgcn_rsqf(fmaxf(s2 * (1.f / 512.f) - mean * mean, 0.f) + 1e-5f);
      f32x4 D[8];
#pragma unroll
      for (int j = 0; j < 8; ++j) D[j] = f32x4{0.f, 0.f, 0.f, 0.f};
#pragma unroll
      for (int s = 0; s < 2; ++s) {
#pragma unroll
        for (int e = 0; e < 4; ++e) xa[s][e] = pk((bflo(xa[s][e]) - mean) * rstd, (bfhi(xa[s][e]) - mean) * rstd);
#pragma unroll
        for (int j = 0; j < 8; ++j) D[j] = mfma16(xa[s], colfrag(wr[s], j), D[j]);
      }
      const unsigned o = WS_QX + (unsigned)((h * 8 + sl) * 2048 + lane * 16);
      st16c(rws, o, u32x4{pk(D[0][0], D[1][0]), tag, pk(D[2][0], D[3][0]), tag});
      st16c(rws, o + 1024, u32x4{pk(D[4][0], D[5][0]), tag, pk(D[6][0], D[7][0]), tag});
      if (lane < 4) *(volatile LAS unsigned*)(L + L_SY + lane * 1024 + 28) = 1u;
    } else {
      if (a < 3) {
#pragma unroll
        for (int i = 0; i < 8; ++i) {
          const int row = 32 * (a - 1) + 4 * i + g;
          const unsigned key = (row & 3) | (((row >> 3) & 3) << 2);
          dma16<true>(wuk, (unsigned)((64 * sl + row) * 4096 + h * 128 + ((n16 ^ key) << 3)) * 2, lbase + L_WK + (8 * (a - 1) + i) * 1024);
        }
      }
      if (a == 3 && lane < 16) st16c(rws, WS_ML + blk * 256 + lane * 16, u32x4{SENT, SENT, SENT, SENT});
      bar();
      if (a == 3) drain();
      for (unsigned it = 0; it < SPIN_CAP * 16; ++it) {
        if (syn[7]) break;
        __builtin_amdgcn_s_sleep(1);
      }
    }
    STAMP(11);
    __builtin_amdgcn_s_sleep(QX_PRE);
    {
      u32x4 t[4];
      for (unsigned it = 0; it < SPIN_CAP; ++it) {
        __builtin_amdgcn_s_sleep(QX_SLEEP);
        fence_c();
#pragma unroll
        for (int i = 0; i < 4; ++i) t[i] = ld16c(rws, WS_QX + (unsigned)((h * 8 + 2 * a) * 2048 + i * 1024 + lane * 16));
        unsigned bad = 0;
#pragma unroll
        for (int i = 0; i < 4; ++i) bad |= (t[i][1] ^ tag) | (t[i][3] ^ tag);
        if (!__any(bad != 0u)) break;
      }
      STAMP(1);
      u32x4 v;
#pragma unroll
      for (int e = 0; e < 4; ++e) {
        const unsigned p = t[e >> 1][2 * (e & 1)], q = t[2 + (e >> 1)][2 * (e & 1)];
        v[e] = pk(bflo(p) + bflo(q), bfhi(p) + bfhi(q));
      }
      LR16(L_QP + a * 1088 + g * 272 + n16 * 16) = v;
    }
    asm volatile("s_waitcnt lgkmcnt(0)" ::: "memory");
    if (lane < 2) *(volatile LAS unsigned*)(L + L_SY + lane * 1024 + a * 4) = 1u;
    if (a < 2) {
      unsigned ko[4];
#pragma unroll
      for (int s = 0; s < 4; ++s) { ko[s] = L_WK + (8 * (n16 >> 2) + 4 * a + (n16 & 3)) * 256 + (((4 * s + g) ^ n16) << 4); asm volatile("" : "+v"(ko[s])); }
      for (unsigned it = 0; it < SPIN_CAP * 16; ++it) {
        const u32x4 f = *(volatile LAS u32x4*)syn;
        if (f[0] & f[1] & f[2] & f[3]) break;
      }
      fence_c();
      STAMP(2);
      u32x4 qp[4][4], wk[2][4];
#pragma unroll
      for (int s = 0; s < 4; ++s) {
#pragma unroll
        for (int pr = 0; pr < 2; ++pr) wk[pr][s] = LR16(ko[s] + pr * 8192);
#pragma unroll
        for (int k = 0; k < 4; ++k) qp[s][k] = LR16(L_QP + (lane & 3) * 272 + g * 16 + k * 1088 + s * 64);
      }
      sb0();
      f32x4 d[2][2] = {{{0.f, 0.f, 0.f, 0.f}, {0.f, 0.f, 0.f, 0.f}}, {{0.f, 0.f, 0.f, 0.f}, {0.f, 0.f, 0.f, 0.f}}};
#pragma unroll
      for (int s = 0; s < 4; ++s)
#pragma unroll
        for (int k = 0; k < 4; ++k)
#pragma unroll
          for (int pr = 0; pr < 2; ++pr) d[pr][k & 1] = mfma16(wk[pr][s], qp[s][k], d[pr][k & 1]);
      const float sc = 0.08838834764831845f * 1.4426950408889634f;
      const f32x4 v = ((n16 & 4) ? d[1][0] + d[1][1] : d[0][0] + d[0][1]) * sc;
      if (n16 < 8)
        st16c(rws, WS_QA + (unsigned)((lane & 3) * 65536 + h * 2048 + a * 1024 + sl * 128 + (n16 >> 2) * 64 + g * 16), u32x4{pk(v[0], v[1]), tag, pk(v[2], v[3]), tag});
      if (lane < 4) *(volatile LAS unsigned*)(L + L_SY + lane * 1024 + 16 + a * 4) = 1u;
      STAMP(3);
    }
    for (unsigned it = 0; it < SPIN_CAP * 16; ++it) {
      if (syn[4] & syn[5]) break;
      __builtin_amdgcn_s_sleep(1);
    }
    __builtin_amdgcn_s_sleep(QA_SLEEP);
    {
      unsigned pend = 0xffu;
      const unsigned qoff = WS_QA + (unsigned)(ab * 65536 + a * 2048 + lane * 16);
      for (unsigned it = 0; it < SPIN_CAP; ++it) {
        u32x4 qv[8][2];
        fence_c();
#pragma unroll
        for (int i = 0; i < 8; ++i)
          if (pend & (1u << i)) {
            if (QA_L2 && it == 0) { qv[i][0] = ld16(rws, qoff + i * 8192); qv[i][1] = ld16(rws, qoff + i * 8192 + 1024); }
            else { qv[i][0] = ld16c(rws, qoff + i * 8192); qv[i][1] = ld16c(rws, qoff + i * 8192 + 1024); }
          }
#pragma unroll
        for (int i = 0; i < 8; ++i)
          if (pend & (1u << i)) {
            LR16(L_Q + (4 * i + a) * 1024 + ((lane ^ ((a << 2) | (i & 3))) << 4)) = u32x4{qv[i][0][0], qv[i][0][2], qv[i][1][0], qv[i][1][2]};
            const unsigned bad = (qv[i][0][1] ^ tag) | (qv[i][0][3] ^ tag) | (qv[i][1][1] ^ tag) | (qv[i][1][3] ^ tag);
            if (!__any(bad != 0u)) pend &= ~(1u << i);
          }
        if (it == 0) STAMP(4);
        if (!pend) break;
        __builtin_amdgcn_s_sleep(4);
      }
    }
    fence_c();
    STAMP(6);
    u32x4 uv[4], wor[8];
    if (WL == -1) {
      const rsrc_t rv = mk(wuv, 512 * 4096 * 2), ro = mk(wo, 4096 * 512 * 2);
#pragma unroll
      for (int i = 0; i < 4; ++i) uv[i] = ld16n(rv, (unsigned)((64 * sl + 16 * a + 4 * i + g) * 4096 + h * 128 + n16 * 8) * 2);
#pragma unroll
      for (int e = 0; e < 8; ++e) wor[e] = ld16n(ro, (unsigned)((h * 128 + sl * 16 + (g & 1) * 8 + e) * 512 + 128 * a + n16 * 8) * 2);
    }
    PV_ADDR(a, 1)
    bar();
    u32x4 va[2][8];
    if (AUXF == 0) { PV_FRAGS(0) PV_FRAGS(1) }
    sb0();
    if (blk == 0 && a == 0 && lane == 0) __builtin_amdgcn_raw_buffer_store_b32(tag, rws, WS_EPOCH, 0, 16);
    unsigned touch = 0;
    if (LATEC == 1) {
      st16c(rws, WS_HB + blk * CLR_PER_WG + a * 1024 + lane * 16, u32x4{SENT, SENT, SENT, SENT});
      if (a < 3) st16c(rws, WS_HB + blk * CLR_PER_WG + (4 + a) * 1024 + lane * 16, u32x4{SENT, SENT, SENT, SENT});
    }
    if (WL == 0 || WL == -2) {
      const rsrc_t rv = mk(wuv, 512 * 4096 * 2), ro = mk(wo, 4096 * 512 * 2);
#pragma unroll
      for (int i = 0; i < 4; ++i) uv[i] = ld16n(rv, (unsigned)((64 * sl + 16 * a + 4 * i + g) * 4096 + h * 128 + n16 * 8) * 2);
#pragma unroll
      for (int e = 0; e < (WL == 0 ? 8 : 0); ++e) wor[e] = ld16n(ro, (unsigned)((h * 128 + sl * 16 + (g & 1) * 8 + e) * 512 + 128 * a + n16 * 8) * 2);
    }
    if (LATEC == 2) {
      st16c(rws, WS_HB + blk * CLR_PER_WG + a * 1024 + lane * 16, u32x4{SENT, SENT, SENT, SENT});
      if (a < 3) st16c(rws, WS_HB + blk * CLR_PER_WG + (4 + a) * 1024 + lane * 16, u32x4{SENT, SENT, SENT, SENT});
    }
    sb0();
    if (AUXF == 1) { PV_FRAGS(0) PV_FRAGS(1) }
    if (AUXF == 4) { PV_FRAGS(0) }
    if (WL >= 2) {
      __builtin_amdgcn_s_sleep(WL);
      const rsrc_t rv = mk(wuv, 512 * 4096 * 2), ro = mk(wo, 4096 * 512 * 2);
#pragma unroll
      for (int i = 0; i < 4; ++i) uv[i] = ld16n(rv, (unsigned)((64 * sl + 16 * a + 4 * i + g) * 4096 + h * 128 + n16 * 8) * 2);
#pragma unroll
      for (int e = 0; e < 8; ++e) wor[e] = ld16n(ro, (unsigned)((h * 128 + sl * 16 + (g & 1) * 8 + e) * 512 + 128 * a + n16 * 8) * 2);
    }
    sb0();
    asm volatile("s_barrier" ::: "memory");
    if (AUXF == 2) { PV_FRAGS(0) PV_FRAGS(1) }
    sb0();
    asm volatile("s_barrier" ::: "memory");
    if (AUXF == 3) { PV_FRAGS(0) PV_FRAGS(1) }
    if (AUXF == 4) { PV_FRAGS(1) }
    sb0();
    if (WL == 1) {
      const rsrc_t rv = mk(wuv, 512 * 4096 * 2), ro = mk(wo, 4096 * 512 * 2);
#pragma unroll
      for (int i = 0; i < 4; ++i) uv[i] = ld16n(rv, (unsigned)((64 * sl + 16 * a + 4 * i + g) * 4096 + h * 128 + n16 * 8) * 2);
#pragma unroll
      for (int e = 0; e < 8; ++e) wor[e] = ld16n(ro, (unsigned)((h * 128 + sl * 16 + (g & 1) * 8 + e) * 512 + 128 * a + n16 * 8) * 2);
    }
    sb0();
    const char *p0, *p1;
    {
      const int xc = blk & 7, id = TOUCH == 4 ? (((blk >> 3) - 12) * 2) & 31 : blk >> 3;
      const unsigned og = (unsigned)((id * 16 + (lane >> 2)) * (DF * 2) + xc * 352 + (lane & 3) * 116);
      const unsigned od = (unsigned)(xc * 176 * 1024 + (id * 44 + (a & 1) * 22 + min(lane, 21)) * 128);
      p0 = (const char*)(a == 0 ? wg : a == 1 ? wu : wd);
      p1 = p0 + min(a < 2 ? og + 16 * DF * 2 : od + 44 * 128, 512u * DF * 2 - 4u);
      p0 += min(a < 2 ? og : od, 512u * DF * 2 - 4u);
    }
#define do_touch() asm volatile("global_load_dword %0, %1, off" : "+v"(touch) : "v"(p0) : "memory")
    if (TOUCH == 1) do_touch();
    bar();
    {
      u32x4 pf[8];
      pv_store(L, rws, va, pf, sw, sr, so, lane);
    }
#pragma unroll
    for (int i = 0; i < 4; ++i) LR16(L_UV + (16 * a + 4 * i + g) * 272 + n16 * 16) = uv[i];
    if (!PSENT) asm volatile("s_waitcnt vmcnt(0)" : "+v"(touch)::"memory");
    bar();
    if (TOUCH == 2) do_touch();
    if (WL == -2) {
      const rsrc_t rv = mk(wuv, 512 * 4096 * 2), ro = mk(wo, 4096 * 512 * 2);
#pragma unroll
      for (int i = 0; i < 0; ++i) uv[i] = ld16n(rv, (unsigned)((64 * sl + 16 * a + 4 * i + g) * 4096 + h * 128 + n16 * 8) * 2);
#pragma unroll
      for (int e = 0; e < 8; ++e) wor[e] = ld16n(ro, (unsigned)((h * 128 + sl * 16 + (g & 1) * 8 + e) * 512 + 128 * a + n16 * 8) * 2);
    }
    u32x4 vf[2][2], of[8];
#pragma unroll
    for (int tt = 0; tt < 2; ++tt)
#pragma unroll
      for (int s = 0; s < 2; ++s) {
        const unsigned ad = L_UV + (32 * s + 8 * g + (n16 >> 2)) * 272 + a * 64 + tt * 32 + (lane & 3) * 8;
        const u32x2 v0 = TR(ad), v1 = TR(ad + 4 * 272);
        vf[tt][s] = u32x4{v0[0], v0[1], v1[0], v1[1]};
        asm volatile("" : "+v"(vf[tt][s]));
      }
#pragma unroll
    for (int j = 0; j < 8; ++j) { of[j] = colfrag(wor, j); asm volatile("" : "+v"(of[j])); }
    unsigned xo = WS_CX + (unsigned)(((h * 8 + 2 * a + ((n16 >> 2) & 1)) * 8 + sl) * 128 + (lane & 3) * 32 + g * 8);
    unsigned xi = WS_CX + (unsigned)(((h * 8 + sl) * 8 + (g >> 1)) * 128 + (n16 >> 2) * 32 + (g & 1) * 16);
    unsigned po = WS_P2 + (unsigned)(g * 8 + 2 * a + (n16 >> 3)) * PST + (unsigned)(blk * 128 + (n16 & 7) * 16);
    asm volatile("" : "+v"(xo), "+v"(xi), "+v"(po));
    STAMP(5);
    bar();
    if (TOUCH == 3) do_touch();
    {
      const u32x4 c0 = LR16(L_CC + (lane & 3) * 128 + g * 16), c1 = LR16(L_CC + (lane & 3) * 128 + 64 + g * 16);
      const f32x4 d0 = mfma16(vf[0][1], c1, mfma16(vf[0][0], c0, f32x4{0.f, 0.f, 0.f, 0.f}));
      const f32x4 d1 = mfma16(vf[1][1], c1, mfma16(vf[1][0], c0, f32x4{0.f, 0.f, 0.f, 0.f}));
      const f32x4 d = (n16 & 4) ? d1 : d0;
      if (n16 < 8) st8c(rws, xo, u32x2{canon2(pk(d[0], d[1])), canon2(pk(d[2], d[3]))});
      u32x4 cf[4];
      const unsigned xis[4] = {xi, xi + 256, xi + 512, xi + 768};
      __builtin_amdgcn_s_sleep(CX_SLEEP);
      poll16<4, 0>(rws, xis, cf);
      if (TOUCH == 2 || TOUCH == 3) asm volatile("s_waitcnt vmcnt(0)" : "+v"(touch)::"memory");
      STAMP(7);
      f32x4 o[8];
#pragma unroll
      for (int j = 0; j < 8; ++j) o[j] = f32x4{0.f, 0.f, 0.f, 0.f};
#pragma unroll
      for (int s = 0; s < 4; ++s)
#pragma unroll
        for (int j = 0; j < 8; ++j) o[j] = mfma16(cf[s], of[j], o[j]);
      st16c(rws, po, u32x4{canon2(pk(o[0][0], o[1][0])), canon2(pk(o[2][0], o[3][0])), canon2(pk(o[4][0], o[5][0])), canon2(pk(o[6][0], o[7][0]))});
    }
    STAMP(8);
    if (TOUCH == 4 && blk >= 96 && blk < 224) {
      unsigned t2 = 0;
      asm volatile("s_barrier\n\tglobal_load_dword %0, %2, off\n\tglobal_load_dword %1, %3, off\n\ts_waitcnt vmcnt(0)" : "+v"(touch), "+v"(t2) : "v"(p0), "v"(p1) : "memory");
    }
    if (ffn) {
      u32x4 gu[8], dn[8], gf[2][4], df[8];
      asm volatile("s_barrier" ::: "memory");
      {
        const rsrc_t rg = mk(wg, 512 * DF * 2), ru = mk(wu, 512 * DF * 2), rd = mk(wd, DF * 512 * 2);
        const unsigned go = (unsigned)((a * 128 + (lane >> 1)) * DF + fi * 16 + (lane & 1) * 8) * 2;
#pragma unroll
        for (int i = 0; i < 8; ++i) gu[i] = ld16(i < 4 ? rg : ru, go + (i & 3) * (32 * DF * 2));
#pragma unroll
        for (int e = 0; e < 8; ++e) dn[e] = ld16(rd, (unsigned)((fi * 16 + (g & 1) * 8 + e) * 512 + a * 128 + n16 * 8) * 2);
        fence_c();
      }
      LAS float* red = (LAS float*)(L + L_RED + a * 704);
      float csum[2];
      {
        const unsigned sb = L_GU + a * 8192;
#pragma unroll
        for (int i = 0; i < 8; ++i) LR16(sb + i * 1024 + lane * 16) = gu[i];
        const u32x4 ones = {0x3f803f80u, 0x3f803f80u, 0x3f803f80u, 0x3f803f80u};
#pragma unroll
        for (int m = 0; m < 2; ++m) {
          f32x4 c = {0.f, 0.f, 0.f, 0.f};
#pragma unroll
          for (int s = 0; s < 4; ++s) {
            const unsigned ad = sb + m * 4096 + (32 * s + 8 * g + (n16 >> 2)) * 32 + (lane & 3) * 8;
            const u32x2 v0 = TR(ad), v1 = TR(ad + 128);
            gf[m][s] = u32x4{v0[0], v0[1], v1[0], v1[1]};
            c = mfma16(ones, gf[m][s], c);
            asm volatile("" : "+v"(gf[m][s]));
          }
          csum[m] = c[0];
        }
#pragma unroll
        for (int j = 0; j < 8; ++j) { df[j] = colfrag(dn, j); asm volatile("" : "+v"(df[j])); }
      }
      if (g < 2) red[128 + g * 16 + n16] = g ? csum[1] : csum[0];
      STAMP(12);
      u32x4 hv[4];
      {
        const unsigned ho = WS_HB + (unsigned)((blk & 7) * 4096 + ((n16 >> 2) * 512 + a * 128 + g * 8) * 2);
        const unsigned hos[4] = {ho, ho + 64, ho + 128, ho + 192};
        poll16<4, HB_SL>(rws, hos, hv);
      }
      STAMP(9);
      {
        f32x4 G = {0.f, 0.f, 0.f, 0.f}, U = {0.f, 0.f, 0.f, 0.f};
        float s1 = 0.f, s2 = 0.f;
#pragma unroll
        for (int s = 0; s < 4; ++s) {
          G = mfma16(hv[s], gf[0][s], G); U = mfma16(hv[s], gf[1][s], U);
#pragma unroll
          for (int e = 0; e < 4; ++e) { s1 = dot2(hv[s][e], 0x3f803f80u, s1); s2 = dot2(hv[s][e], hv[s][e], s2); }
        }
        s1 = sum32(sum16(s1)); s2 = sum32(sum16(s2));
        red[lane] = G[0];
        red[64 + lane] = U[0];
        if (lane < 16) { red[160 + (lane >> 2) * 2] = s1; red[161 + (lane >> 2) * 2] = s2; }
      }
      bar();
      float G = 0.f, U = 0.f, cg = 0.f, cu = 0.f, s1 = 0.f, s2 = 0.f;
#pragma unroll
      for (int k = 0; k < 4; ++k) {
        LAS float* rk = (LAS float*)(L + L_RED + k * 704);
        G += rk[lane]; U += rk[64 + lane]; cg += rk[128 + n16]; cu += rk[144 + n16];
        s1 += rk[160 + g * 2]; s2 += rk[161 + g * 2];
      }
      const float mean = s1 * (1.f / 512.f);
      const float var = fmaxf(s2 * (1.f / 512.f) - mean * mean, 0.f);
      const float rstd = __builtin_amdgcn_rsqf(var + 1e-5f);
      const float gg = (G - mean * cg) * rstd, uu = (U - mean * cu) * rstd;
      const float z = 0.7978845608028654f * (gg + 0.044715f * gg * gg * gg);
      const float th = 1.f - 2.f * __builtin_amdgcn_rcpf(1.f + __expf(2.f * z));
      const float hh = 0.5f * gg * (1.f + th) * uu;
      *(LAS uint16_t*)(L + L_HS + a * 128 + lane * 2) = (uint16_t)pk(hh, 0.f);
      fence_c();
      u32x4 ha = LR16(L_HS + a * 128 + (n16 >> 2) * 32 + (g & 1) * 16);
      if (g >= 2) ha = u32x4{0u, 0u, 0u, 0u};
      float o[8];
#pragma unroll
      for (int j = 0; j < 8; ++j) o[j] = mfma16(ha, df[j], f32x4{0.f, 0.f, 0.f, 0.f})[0];
      st16c(rws, WS_P4 + (unsigned)(((g * 8 + 2 * a + (n16 >> 3)) * NFFN + fi) * 128 + (n16 & 7) * 16),
            u32x4{canon2(pk(o[0], o[1])), canon2(pk(o[2], o[3])), canon2(pk(o[4], o[5])), canon2(pk(o[6], o[7]))});
      STAMP(10);
    }
    STAMP_FLUSH;
  }
}

extern "C" void kernel_launch(void* const* d_in, const int* in_sizes, int n_in, void* d_out, int out_size, void* d_ws,
                              size_t ws_size, hipStream_t stream) {
  fused_kernel<<<256, 512, 0, stream>>>((const uint16_t*)d_in[0], (const uint16_t*)d_in[1], (const uint16_t*)d_in[2],
                                        (const uint16_t*)d_in[3], (const uint16_t*)d_in[4], (const uint16_t*)d_in[5],
                                        (const uint16_t*)d_in[7], (const uint16_t*)d_in[8], (const uint16_t*)d_in[9],
                                        (uint16_t*)d_out, (char*)d_ws);
}
```
